```python
import torch

def _softmax(x, axis=-1):
    return torch.softmax(x, dim=axis)
def _gelu(x):
    return torch.nn.functional.gelu(x, approximate="tanh")
def _silu(x):
    return torch.nn.functional.silu(x)
def _sigmoid(x):
    return torch.sigmoid(x)
def _layernorm(x, axis=-1, eps=1e-5):
    m = x.mean(dim=axis, keepdim=True)
    v = x.var(dim=axis, keepdim=True, unbiased=False)
    return (x - m) / torch.sqrt(v + eps)
def _scatter_add(tgt, idx, src, axis=0):
    return tgt.clone().index_add_(axis, idx, src)
def _segment_sum(src, seg, n):
    out = torch.zeros((n,) + src.shape[1:], dtype=src.dtype)
    return out.index_add_(0, seg, src)
def _pad(x, pw):
    flat = tuple(v for pair in reversed(pw) for v in pair)
    return torch.nn.functional.pad(x, flat)

kernel_name = 'gated_mix__ssm_conv_s5__dense_activation_gelu__prompt'

def setup_inputs():

    _w = (lambda a: (lambda t: t.to(torch.bfloat16) if t.is_floating_point() else t)(torch.as_tensor(a)))
    def _A_setup():
        B, S, D = 1, 2048, 512
        P = D
        Lambda_re = -0.5 * (lambda _x: torch.exp(torch.as_tensor(_x)))((lambda *s: torch.randn(*s, dtype=torch.bfloat16) * 0.2)(P))
        Lambda_im = (lambda *s: torch.randn(*s, dtype=torch.bfloat16) * 0.8)(P) * 1.0
        B_re = (lambda *s: torch.randn(*s, dtype=torch.bfloat16) * 0.2)(P, D) * (D ** -0.5)
        B_im = (lambda *s: torch.randn(*s, dtype=torch.bfloat16) * 0.2)(P, D) * (D ** -0.5)
        C_re = (lambda *s: torch.randn(*s, dtype=torch.bfloat16) * 0.2)(D, P) * (P ** -0.5)
        C_im = (lambda *s: torch.randn(*s, dtype=torch.bfloat16) * 0.2)(D, P) * (P ** -0.5)
        log_dt = (lambda *s: torch.randn(*s, dtype=torch.bfloat16) * 0.2)(P) * 0.3 - 2.0
        Dskip = (lambda *s: torch.randn(*s, dtype=torch.bfloat16) * 0.05)(D)
        return {
            "x": _w((lambda *s: torch.randn(*s, dtype=torch.bfloat16) * 0.05)(B, S, D)),
            "Lambda_re": _w(Lambda_re),
            "Lambda_im": _w(Lambda_im),
            "B_re": _w(B_re),
            "B_im": _w(B_im),
            "C_re": _w(C_re),
            "C_im": _w(C_im),
            "log_dt": _w(log_dt),
            "Dskip": _w(Dskip),
        }
    _A_in = _A_setup()

    def _B_setup():
        B, S, D, F = 1, 2048, 512, 1792
        return {
            "x": _w((lambda *s: torch.randn(*s, dtype=torch.bfloat16) * 0.05)(B, S, D)),
            "w1": _w((lambda *s: torch.randn(*s, dtype=torch.bfloat16) * 3.2)(D, F) * (D ** -0.5)),
            "b1": _w((lambda *s: torch.randn(*s, dtype=torch.bfloat16) * 3.2)(F) * 0.01),
            "w2": _w((lambda *s: torch.randn(*s, dtype=torch.bfloat16) * 0.1)(F, D) * (F ** -0.5)),
            "b2": _w((lambda *s: torch.randn(*s, dtype=torch.bfloat16) * 0.2)(D) * 0.01),
        }
    _B_in = _B_setup()

    return {
        "A_x": _A_in["x"],
        "A_Lambda_re": _A_in["Lambda_re"],
        "A_Lambda_im": _A_in["Lambda_im"],
        "A_B_re": _A_in["B_re"],
        "A_B_im": _A_in["B_im"],
        "A_C_re": _A_in["C_re"],
        "A_C_im": _A_in["C_im"],
        "A_log_dt": _A_in["log_dt"],
        "A_Dskip": _A_in["Dskip"],
        "B_x": _B_in["x"],
        "B_w1": _B_in["w1"],
        "B_b1": _B_in["b1"],
        "B_w2": _B_in["w2"],
        "B_b2": _B_in["b2"],
        "gate_w": _w((lambda *s: torch.randn(*s, dtype=torch.bfloat16) * 0.05)(512) * 16.0),
    }

def reference(A_x, A_Lambda_re, A_Lambda_im, A_B_re, A_B_im, A_C_re, A_C_im, A_log_dt, A_Dskip, B_x, B_w1, B_b1, B_w2, B_b2, gate_w):
    def _A_ref(x, Lambda_re, Lambda_im, B_re, B_im, C_re, C_im, log_dt, Dskip):
        Bsz, L, H = x.shape
        Lam = Lambda_re + 1j * Lambda_im
        Bmat = B_re + 1j * B_im
        Cmat = C_re + 1j * C_im
        dt = torch.exp(log_dt)
        Lam_bar = torch.exp(dt * Lam)
        B_bar = ((Lam_bar - 1.0) / Lam)[:, None] * Bmat
        Bu = torch.einsum("bld,pd->blp", x + 0j, B_bar)
        h = torch.zeros_like(Bu[:, 0, :])
        hs = []
        for t in range(L):
            h = Lam_bar[None, :] * h + Bu[:, t, :]
            hs.append(h)
        hseq = torch.stack(hs, 1)
        y = 2.0 * torch.einsum("blp,dp->bld", hseq, Cmat).real + x * Dskip[None, None, :]
        return y

    def _B_ref(x, w1, b1, w2, b2):
        h = torch.einsum("bsd,df->bsf", x, w1) + b1
        h = _gelu(h)
        return torch.einsum("bsf,fd->bsd", h, w2) + b2

    x = A_x
    g = _sigmoid((x * gate_w).sum(-1, keepdims=True))
    a = _A_ref(x, A_Lambda_re, A_Lambda_im, A_B_re, A_B_im, A_C_re, A_C_im, A_log_dt, A_Dskip)
    b = _B_ref(x, B_w1, B_b1, B_w2, B_b2)
    return g * a + (1 - g) * b
```

```cpp
#include <hip/hip_runtime.h>
#include <hip/hip_bf16.h>
#include <stdio.h>

typedef __hip_bfloat16 bf16;
#define L_ 2048
#define D_ 512
#define P_ 512
#define F_ 1792

__device__ __forceinline__ float b2f(bf16 v) { return __bfloat162float(v); }

__global__ void k_bu(const bf16* x, const bf16* lre, const bf16* lim, const bf16* bre, const bf16* bim, const bf16* logdt, float2* Bu) {
    int p = blockIdx.x * blockDim.x + threadIdx.x; int l = blockIdx.y;
    if (p >= P_) return;
    float sr = 0, si = 0;
    for (int d = 0; d < D_; ++d) { float xv = b2f(x[l * D_ + d]); sr += xv * b2f(bre[p * D_ + d]); si += xv * b2f(bim[p * D_ + d]); }
    float dt = expf(b2f(logdt[p])); float ar = b2f(lre[p]), ai = b2f(lim[p]);
    float e = expf(dt * ar); float lbr = e * cosf(dt * ai), lbi = e * sinf(dt * ai);
    float nr = lbr - 1.f, ni = lbi; float den = ar * ar + ai * ai;
    float cr = (nr * ar + ni * ai) / den, ci = (ni * ar - nr * ai) / den;
    Bu[l * P_ + p] = make_float2(cr * sr - ci * si, cr * si + ci * sr);
}
__global__ void k_scan(const bf16* lre, const bf16* lim, const bf16* logdt, float2* Bu) {
    int p = blockIdx.x * blockDim.x + threadIdx.x; if (p >= P_) return;
    float dt = expf(b2f(logdt[p])); float ar = b2f(lre[p]), ai = b2f(lim[p]);
    float e = expf(dt * ar); float lbr = e * cosf(dt * ai), lbi = e * sinf(dt * ai);
    float hr = 0, hi = 0;
    for (int l = 0; l < L_; ++l) { float2 u = Bu[l * P_ + p]; float nr = lbr * hr - lbi * hi + u.x; float ni = lbr * hi + lbi * hr + u.y; hr = nr; hi = ni; Bu[l * P_ + p] = make_float2(hr, hi); }
}
__global__ void k_h(const bf16* x, const bf16* w1, const bf16* b1, float* H) {
    int f = blockIdx.x * blockDim.x + threadIdx.x; int l = blockIdx.y; if (f >= F_) return;
    float s = 0; for (int d = 0; d < D_; ++d) s += b2f(x[l * D_ + d]) * b2f(w1[d * F_ + f]);
    s += b2f(b1[f]);
    float z = 0.7978845608028654f * (s + 0.044715f * s * s * s);
    H[l * F_ + f] = 0.5f * s * (1.f + tanhf(z));
}
__global__ void k_out(const bf16* x, const bf16* cre, const bf16* cim, const bf16* dskip, const bf16* w2, const bf16* b2, const bf16* gw,
                      const float2* h, const float* H, float* out) {
    int d = blockIdx.x * blockDim.x + threadIdx.x; int l = blockIdx.y; if (d >= D_) return;
    float gs = 0; for (int k = 0; k < D_; ++k) gs += b2f(x[l * D_ + k]) * b2f(gw[k]);
    float g = 1.f / (1.f + expf(-gs));
    float a = 0; for (int p = 0; p < P_; ++p) { float2 hv = h[l * P_ + p]; a += hv.x * b2f(cre[d * P_ + p]) - hv.y * b2f(cim[d * P_ + p]); }
    a = 2.f * a + b2f(x[l * D_ + d]) * b2f(dskip[d]);
    float b = 0; for (int f = 0; f < F_; ++f) b += H[l * F_ + f] * b2f(w2[f * D_ + d]);
    b += b2f(b2[d]);
    out[l * D_ + d] = g * a + (1.f - g) * b;
}

extern "C" void kernel_launch(void* const* d_in, const int* in_sizes, int n_in,
                              void* d_out, int out_size, void* d_ws, size_t ws_size,
                              hipStream_t stream) {
    const bf16* x = (const bf16*)d_in[0]; const bf16* lre = (const bf16*)d_in[1]; const bf16* lim = (const bf16*)d_in[2];
    const bf16* bre = (const bf16*)d_in[3]; const bf16* bim = (const bf16*)d_in[4]; const bf16* cre = (const bf16*)d_in[5];
    const bf16* cim = (const bf16*)d_in[6]; const bf16* logdt = (const bf16*)d_in[7]; const bf16* dskip = (const bf16*)d_in[8];
    const bf16* w1 = (const bf16*)d_in[10]; const bf16* b1 = (const bf16*)d_in[11]; const bf16* w2 = (const bf16*)d_in[12];
    const bf16* b2 = (const bf16*)d_in[13]; const bf16* gw = (const bf16*)d_in[14];
    float2* Bu = (float2*)d_ws; float* H = (float*)((char*)d_ws + (size_t)L_ * P_ * 8);
    k_bu<<<dim3(P_ / 256, L_), 256, 0, stream>>>(x, lre, lim, bre, bim, logdt, Bu);
    k_scan<<<P_ / 64, 64, 0, stream>>>(lre, lim, logdt, Bu);
    k_h<<<dim3(F_ / 256, L_), 256, 0, stream>>>(x, w1, b1, H);
    k_out<<<dim3(D_ / 256, L_), 256, 0, stream>>>(x, cre, cim, dskip, w2, b2, gw, Bu, H, (float*)d_out);
}
```

```cpp
#include <hip/hip_runtime.h>
#include <hip/hip_bf16.h>

typedef __hip_bfloat16 bf16;
typedef __attribute__((ext_vector_type(8))) short bf16x8;
typedef __attribute__((ext_vector_type(4))) short bf16x4;
typedef __attribute__((ext_vector_type(4))) float f32x4;
typedef __attribute__((ext_vector_type(2))) float f32x2;
typedef __attribute__((address_space(3))) bf16x4 lds_bf16x4;

constexpr int L_ = 2048, D_ = 512, P_ = 512, F_ = 1792;
constexpr int T_ = 128;
constexpr int NCH = L_ / T_;

#define WAIT_VM(n) asm volatile("s_waitcnt vmcnt(" #n ")" ::: "memory")
#define WAIT_LGKM0() asm volatile("s_waitcnt lgkmcnt(0)" ::: "memory")
#define BAR() __builtin_amdgcn_s_barrier()

__device__ __forceinline__ float bf2f(unsigned short u) { return __uint_as_float(((unsigned)u) << 16); }
__device__ __forceinline__ unsigned short f2bf(float f) {
    bf16 b = __float2bfloat16(f);
    return *reinterpret_cast<unsigned short*>(&b);
}
__device__ __forceinline__ unsigned pack2(float lo, float hi) { return (unsigned)f2bf(lo) | ((unsigned)f2bf(hi) << 16); }
__device__ __forceinline__ float ldbf(const bf16* p) { return bf2f(*reinterpret_cast<const unsigned short*>(p)); }

__device__ __forceinline__ void glds16(const void* g, void* l) {
    __builtin_amdgcn_global_load_lds((const unsigned*)g, (unsigned*)l, 16, 0, 0);
}

__device__ __forceinline__ f32x2 lam_bar(float lre, float lim, float dt) {
    float e = expf(dt * lre);
    float s, c;
    sincosf(dt * lim, &s, &c);
    f32x2 r; r.x = e * c; r.y = e * s; return r;
}
__device__ __forceinline__ f32x2 cmul(f32x2 a, f32x2 b) {
    f32x2 r; r.x = a.x * b.x - a.y * b.y; r.y = a.x * b.y + a.y * b.x; return r;
}

constexpr int K1_A = 128 * 128;
constexpr int K1_BS = 64 * 128;
constexpr int K1_BF = 64 * 112 * 2;
constexpr int K1_STAGE = K1_A + K1_BS + K1_BF;
constexpr int K1_NST = 4;
constexpr int K1_LDS = K1_STAGE * K1_NST;

__global__ __launch_bounds__(256, 1) void k1_gemm_scan(
    const bf16* __restrict__ x, const bf16* __restrict__ lre, const bf16* __restrict__ lim,
    const bf16* __restrict__ bre, const bf16* __restrict__ bim, const bf16* __restrict__ logdt,
    const bf16* __restrict__ w1, const bf16* __restrict__ b1,
    unsigned* __restrict__ hloc, float* __restrict__ agg, bf16* __restrict__ Hout)
{
    extern __shared__ __attribute__((aligned(16))) char smem[];
    const int tid = threadIdx.x, lane = tid & 63;
    const int w = __builtin_amdgcn_readfirstlane(tid >> 6);
    const int xcd = blockIdx.x & 7, ii = blockIdx.x >> 3;
    const int rp = (xcd & 1) * 8 + (ii & 7);
    const int cg = (xcd >> 1) * 4 + (ii >> 3);
    const int l0 = rp * T_, p0 = cg * 32, f0 = cg * 112;

    const bf16* srcA[4]; const bf16* srcS[2]; const bf16* srcF[4];
#pragma unroll
    for (int i = 0; i < 4; ++i) {
        int row = (w * 4 + i) * 8 + (lane >> 3);
        int ch = (lane & 7) ^ ((row >> 1) & 7);
        srcA[i] = x + (size_t)(l0 + row) * D_ + ch * 8;
    }
#pragma unroll
    for (int i = 0; i < 2; ++i) {
        int n = (w * 2 + i) * 8 + (lane >> 3);
        int ch = (lane & 7) ^ ((n >> 1) & 7);
        const bf16* base = (n < 32) ? bre : bim;
        srcS[i] = base + (size_t)(p0 + (n & 31)) * D_ + ch * 8;
    }
    const int fbase = (w < 2) ? w * 4 : 8 + (w - 2) * 3;
#pragma unroll
    for (int i = 0; i < 4; ++i) {
        int ins = fbase + i; if (ins > 13) ins = 13;
        int st = ins * 4 + (lane >> 4);
        int kb = st / 7, nb = st - kb * 7;
        int prow = (lane >> 1) & 7, half = lane & 1;
        int k = kb * 8 + (prow ^ ((kb & 1) * 4));
        srcF[i] = w1 + (size_t)k * F_ + f0 + nb * 16 + half * 8;
    }

    auto issue = [&](int kt) {
        char* sb = smem + (kt & (K1_NST - 1)) * K1_STAGE;
        const int ko = kt * 64;
#pragma unroll
        for (int i = 0; i < 4; ++i) glds16(srcA[i] + ko, sb + (w * 4 + i) * 1024);
#pragma unroll
        for (int i = 0; i < 2; ++i) glds16(srcS[i] + ko, sb + K1_A + (w * 2 + i) * 1024);
#pragma unroll
        for (int i = 0; i < 3; ++i) glds16(srcF[i] + (size_t)ko * F_, sb + K1_A + K1_BS + (fbase + i) * 1024);
        if (w < 2) glds16(srcF[3] + (size_t)ko * F_, sb + K1_A + K1_BS + (fbase + 3) * 1024);
    };

    f32x4 acc[2][11];
#pragma unroll
    for (int m = 0; m < 2; ++m)
#pragma unroll
        for (int n = 0; n < 11; ++n) acc[m][n] = (f32x4){0.f, 0.f, 0.f, 0.f};

    const int fr = lane & 15, fq = lane >> 4;
    int offA[2][2], offS[4][2], offF[2][2];
#pragma unroll
    for (int m = 0; m < 2; ++m)
#pragma unroll
        for (int ks = 0; ks < 2; ++ks) {
            int row = w * 32 + m * 16 + fr; int ch = ks * 4 + fq;
            offA[m][ks] = row * 128 + ((ch ^ ((row >> 1) & 7)) << 4);
        }
#pragma unroll
    for (int n = 0; n < 4; ++n)
#pragma unroll
        for (int ks = 0; ks < 2; ++ks) {
            int row = n * 16 + fr; int ch = ks * 4 + fq;
            offS[n][ks] = K1_A + row * 128 + ((ch ^ ((row >> 1) & 7)) << 4);
        }
#pragma unroll
    for (int ks = 0; ks < 2; ++ks)
#pragma unroll
        for (int t = 0; t < 2; ++t) {
            int kb = ks * 4 + fq; int q = (lane & 15) >> 2, pp = lane & 3;
            offF[ks][t] = K1_A + K1_BS + kb * 7 * 256 + (((4 * t + q) ^ ((kb & 1) * 4)) << 5) + pp * 8;
        }

    auto compute = [&](int kt) {
        const char* sb = smem + (kt & (K1_NST - 1)) * K1_STAGE;
#pragma unroll
        for (int ks = 0; ks < 2; ++ks) {
            bf16x8 xa[2];
#pragma unroll
            for (int m = 0; m < 2; ++m) xa[m] = *reinterpret_cast<const bf16x8*>(sb + offA[m][ks]);
#pragma unroll
            for (int n = 0; n < 4; ++n) {
                bf16x8 wb = *reinterpret_cast<const bf16x8*>(sb + offS[n][ks]);
#pragma unroll
                for (int m = 0; m < 2; ++m) acc[m][n] = __builtin_amdgcn_mfma_f32_16x16x32_bf16(wb, xa[m], acc[m][n], 0, 0, 0);
            }
#pragma unroll
            for (int n = 0; n < 7; ++n) {
                bf16x4 lo = __builtin_amdgcn_ds_read_tr16_b64_v4i16((lds_bf16x4*)(sb + offF[ks][0] + n * 256));
                bf16x4 hi = __builtin_amdgcn_ds_read_tr16_b64_v4i16((lds_bf16x4*)(sb + offF[ks][1] + n * 256));
                bf16x8 wb = __builtin_shufflevector(lo, hi, 0, 1, 2, 3, 4, 5, 6, 7);
#pragma unroll
                for (int m = 0; m < 2; ++m) acc[m][4 + n] = __builtin_amdgcn_mfma_f32_16x16x32_bf16(wb, xa[m], acc[m][4 + n], 0, 0, 0);
            }
        }
    };

    issue(0); issue(1); issue(2);
#define K1_STEP(it, NA, NB)                                   \
    do {                                                      \
        if (w < 2) { WAIT_VM(NA); } else { WAIT_VM(NB); }     \
        BAR();                                                \
        if ((it) + 3 < 8) issue((it) + 3);                    \
        compute(it);                                          \
        WAIT_LGKM0();                                         \
    } while (0)
    K1_STEP(0, 20, 18); K1_STEP(1, 20, 18); K1_STEP(2, 20, 18); K1_STEP(3, 20, 18);
    K1_STEP(4, 20, 18); K1_STEP(5, 20, 18); K1_STEP(6, 10, 9); K1_STEP(7, 0, 0);
#undef K1_STEP
    __syncthreads();

    constexpr int US = 272;
    char* ubuf = smem;
    float* fin = reinterpret_cast<float*>(smem + 36864);
#pragma unroll
    for (int m = 0; m < 2; ++m)
#pragma unroll
        for (int nt = 0; nt < 2; ++nt) {
            int l = w * 32 + m * 16 + fr; int pl = nt * 16 + fq * 4;
            f32x4 re = acc[m][nt], im = acc[m][nt + 2];
            f32x4 v0 = {re[0], im[0], re[1], im[1]}, v1 = {re[2], im[2], re[3], im[3]};
            *reinterpret_cast<f32x4*>(ubuf + l * US + pl * 8) = v0;
            *reinterpret_cast<f32x4*>(ubuf + l * US + pl * 8 + 16) = v1;
        }
#pragma unroll
    for (int n = 0; n < 7; ++n) {
        int f = f0 + n * 16 + fq * 4;
        uint2 bb = *reinterpret_cast<const uint2*>(b1 + f);
        float bias[4] = {bf2f(bb.x & 0xffff), bf2f(bb.x >> 16), bf2f(bb.y & 0xffff), bf2f(bb.y >> 16)};
#pragma unroll
        for (int m = 0; m < 2; ++m) {
            int l = l0 + w * 32 + m * 16 + fr;
            float o[4];
#pragma unroll
            for (int j = 0; j < 4; ++j) {
                float v = acc[m][4 + n][j] + bias[j];
                float z = 0.7978845608028654f * (v + 0.044715f * v * v * v);
                float t = 1.f - 2.f / (__expf(2.f * z) + 1.f);
                o[j] = 0.5f * v * (1.f + t);
            }
            uint2 pk; pk.x = pack2(o[0], o[1]); pk.y = pack2(o[2], o[3]);
            *reinterpret_cast<uint2*>(Hout + (size_t)l * F_ + f) = pk;
        }
    }
    __syncthreads();
    {
        const int pl = tid & 31, seg = tid >> 5, p = p0 + pl;
        const float ar = ldbf(lre + p), ai = ldbf(lim + p), dt = expf(ldbf(logdt + p));
        const f32x2 lam = lam_bar(ar, ai, dt);
        f32x2 coef;
        {
            float nr = lam.x - 1.f, ni = lam.y, den = ar * ar + ai * ai;
            coef.x = (nr * ar + ni * ai) / den; coef.y = (ni * ar - nr * ai) / den;
        }
        f32x2 hv[16]; f32x2 h = {0.f, 0.f};
#pragma unroll
        for (int i = 0; i < 16; ++i) {
            f32x2 raw = *reinterpret_cast<const f32x2*>(ubuf + (seg * 16 + i) * US + pl * 8);
            f32x2 u = cmul(coef, raw);
            f32x2 t = cmul(lam, h);
            h.x = t.x + u.x; h.y = t.y + u.y; hv[i] = h;
        }
        *reinterpret_cast<f32x2*>(fin + (seg * 32 + pl) * 2) = h;
        __syncthreads();
        f32x2 l2 = cmul(lam, lam), l4 = cmul(l2, l2), l8 = cmul(l4, l4), l16 = cmul(l8, l8);
        f32x2 c = {0.f, 0.f};
        for (int s = 0; s < seg; ++s) {
            f32x2 fv = *reinterpret_cast<const f32x2*>(fin + (s * 32 + pl) * 2);
            f32x2 t = cmul(l16, c); c.x = t.x + fv.x; c.y = t.y + fv.y;
        }
        f32x2 t = cmul(c, lam);
#pragma unroll
        for (int i = 0; i < 16; ++i) {
            hv[i].x += t.x; hv[i].y += t.y; t = cmul(t, lam);
            hloc[(size_t)(l0 + seg * 16 + i) * P_ + p] = pack2(hv[i].x, hv[i].y);
        }
        if (seg == 7) *reinterpret_cast<f32x2*>(agg + ((size_t)rp * P_ + p) * 2) = hv[15];
    }
}

constexpr int K2_STAGE = 16384;
constexpr int K2_NST = 6;
constexpr int K2_AP = K2_STAGE * K2_NST;
constexpr int K2_T0 = K2_AP + 8192;
constexpr int K2_LAM = K2_T0 + 32768;
constexpr int K2_G = K2_LAM + 4096;
constexpr int K2_LDS = K2_G + 256;
constexpr int K2_NF = F_ / 64;
constexpr int K2_NS = P_ / 32;
constexpr int K2_NIT = K2_NF + K2_NS;

__global__ __launch_bounds__(256, 1) void k2_gemm_mix(
    const bf16* __restrict__ x, const bf16* __restrict__ lre, const bf16* __restrict__ lim,
    const bf16* __restrict__ cre, const bf16* __restrict__ cim, const bf16* __restrict__ logdt,
    const bf16* __restrict__ dskip, const bf16* __restrict__ w2, const bf16* __restrict__ b2,
    const bf16* __restrict__ gw, const unsigned* __restrict__ hloc, const float* __restrict__ agg,
    const bf16* __restrict__ Hin, float* __restrict__ out)
{
    extern __shared__ __attribute__((aligned(16))) char smem[];
    const int tid = threadIdx.x, lane = tid & 63;
    const int w = __builtin_amdgcn_readfirstlane(tid >> 6);
    const int xcd = blockIdx.x & 7, ii = blockIdx.x >> 3;
    const int rt = (xcd & 3) * 8 + (ii & 7);
    const int ct = (xcd >> 2) * 4 + (ii >> 3);
    const int l0 = rt * 64, d0 = ct * 64;
    const int chunk = rt >> 1, jbase = (rt & 1) * 64 + 1;

    const bf16* srcHA[2]; const bf16* srcW[2]; const unsigned* srcR[2]; const bf16* srcC[2];
#pragma unroll
    for (int i = 0; i < 2; ++i) {
        int row = (w * 2 + i) * 8 + (lane >> 3);
        int ch = (lane & 7) ^ ((row >> 1) & 7);
        srcHA[i] = Hin + (size_t)(l0 + row) * F_ + ch * 8;
        srcR[i] = hloc + (size_t)(l0 + row) * P_ + (lane & 7) * 4;
        srcC[i] = ((ch < 4) ? cre : cim) + (size_t)(d0 + row) * P_ + (ch & 3) * 8;
        int st = (w * 2 + i) * 4 + (lane >> 4);
        int kb = st >> 2, nb = st & 3;
        int prow = (lane >> 1) & 7, half = lane & 1;
        int k = kb * 8 + (prow ^ ((kb & 1) * 4));
        srcW[i] = w2 + (size_t)k * D_ + d0 + nb * 16 + half * 8;
    }
    auto issue = [&](int it) {
        char* sb = smem + (it % K2_NST) * K2_STAGE;
        if (it < K2_NF) {
#pragma unroll
            for (int i = 0; i < 2; ++i) glds16(srcHA[i] + it * 64, sb + (w * 2 + i) * 1024);
#pragma unroll
            for (int i = 0; i < 2; ++i) glds16(srcW[i] + (size_t)it * 64 * D_, sb + 8192 + (w * 2 + i) * 1024);
        } else {
            int s = it - K2_NF;
#pragma unroll
            for (int i = 0; i < 2; ++i) glds16(srcR[i] + s * 32, sb + (w * 2 + i) * 1024);
#pragma unroll
            for (int i = 0; i < 2; ++i) glds16(srcC[i] + s * 32, sb + 8192 + (w * 2 + i) * 1024);
        }
    };

    {
        f32x2* T0 = reinterpret_cast<f32x2*>(smem + K2_T0);
        f32x2* lamt = reinterpret_cast<f32x2*>(smem + K2_LAM);
#pragma unroll
        for (int h = 0; h < 2; ++h) {
            const int p = tid + h * 256;
            const float ar = ldbf(lre + p), ai = ldbf(lim + p), dt = expf(ldbf(logdt + p));
            const f32x2 lam = lam_bar(ar, ai, dt);
            f32x2 l2 = cmul(lam, lam), l4 = cmul(l2, l2), l8 = cmul(l4, l4), l16 = cmul(l8, l8);
            f32x2 l32 = cmul(l16, l16), l64 = cmul(l32, l32), l128 = cmul(l64, l64);
            f32x2 S = {0.f, 0.f};
            for (int c = 0; c < chunk; ++c) {
                f32x2 a = *reinterpret_cast<const f32x2*>(agg + ((size_t)c * P_ + p) * 2);
                f32x2 t = cmul(l128, S); S.x = t.x + a.x; S.y = t.y + a.y;
            }
            f32x2 t = cmul(S, lam);
            if (jbase > 1) t = cmul(t, l64);
#pragma unroll
            for (int rg = 0; rg < 8; ++rg) { T0[rg * P_ + p] = t; t = cmul(t, l8); }
            lamt[p] = lam;
        }
        float* gs = reinterpret_cast<float*>(smem + K2_G);
        uint4 gwv = *reinterpret_cast<const uint4*>(gw + lane * 8);
        float gwf[8] = {bf2f(gwv.x & 0xffff), bf2f(gwv.x >> 16), bf2f(gwv.y & 0xffff), bf2f(gwv.y >> 16),
                        bf2f(gwv.z & 0xffff), bf2f(gwv.z >> 16), bf2f(gwv.w & 0xffff), bf2f(gwv.w >> 16)};
        for (int r = 0; r < 16; ++r) {
            int row = w * 16 + r;
            uint4 xv = *reinterpret_cast<const uint4*>(x + (size_t)(l0 + row) * D_ + lane * 8);
            float s = bf2f(xv.x & 0xffff) * gwf[0] + bf2f(xv.x >> 16) * gwf[1] + bf2f(xv.y & 0xffff) * gwf[2] + bf2f(xv.y >> 16) * gwf[3]
                    + bf2f(xv.z & 0xffff) * gwf[4] + bf2f(xv.z >> 16) * gwf[5] + bf2f(xv.w & 0xffff) * gwf[6] + bf2f(xv.w >> 16) * gwf[7];
#pragma unroll
            for (int o = 32; o > 0; o >>= 1) s += __shfl_xor(s, o);
            if (lane == 0) gs[row] = 1.f / (1.f + __expf(-s));
        }
    }
    asm volatile("s_waitcnt vmcnt(0) lgkmcnt(0)" ::: "memory");

#pragma unroll
    for (int i = 0; i < K2_NST - 1; ++i) issue(i);

    const int fr = lane & 15, fq = lane >> 4;
    const int wm = w >> 1, wn = w & 1;
    int offA[2][2], offB[2][2], offW[2][2];
#pragma unroll
    for (int m = 0; m < 2; ++m)
#pragma unroll
        for (int ks = 0; ks < 2; ++ks) {
            int row = wm * 32 + m * 16 + fr; int ch = ks * 4 + fq;
            offA[m][ks] = row * 128 + ((ch ^ ((row >> 1) & 7)) << 4);
            int rowb = wn * 32 + m * 16 + fr;
            offB[m][ks] = 8192 + rowb * 128 + ((ch ^ ((rowb >> 1) & 7)) << 4);
        }
#pragma unroll
    for (int ks = 0; ks < 2; ++ks)
#pragma unroll
        for (int t = 0; t < 2; ++t) {
            int kb = ks * 4 + fq; int q = (lane & 15) >> 2, pp = lane & 3;
            offW[ks][t] = 8192 + (kb * 4 + wn * 2) * 256 + (((4 * t + q) ^ ((kb & 1) * 4)) << 5) + pp * 8;
        }

    f32x4 accf[2][2], accs[2][2];
#pragma unroll
    for (int m = 0; m < 2; ++m)
#pragma unroll
        for (int n = 0; n < 2; ++n) { accf[m][n] = (f32x4){0.f, 0.f, 0.f, 0.f}; accs[m][n] = (f32x4){0.f, 0.f, 0.f, 0.f}; }

    for (int it = 0; it < K2_NF; ++it) {
        WAIT_VM(16);
        BAR();
        issue(it + K2_NST - 1);
        const char* sb = smem + (it % K2_NST) * K2_STAGE;
#pragma unroll
        for (int ks = 0; ks < 2; ++ks) {
            bf16x8 ha[2];
#pragma unroll
            for (int m = 0; m < 2; ++m) ha[m] = *reinterpret_cast<const bf16x8*>(sb + offA[m][ks]);
#pragma unroll
            for (int n = 0; n < 2; ++n) {
                bf16x4 lo = __builtin_amdgcn_ds_read_tr16_b64_v4i16((lds_bf16x4*)(sb + offW[ks][0] + n * 256));
                bf16x4 hi = __builtin_amdgcn_ds_read_tr16_b64_v4i16((lds_bf16x4*)(sb + offW[ks][1] + n * 256));
                bf16x8 wb = __builtin_shufflevector(lo, hi, 0, 1, 2, 3, 4, 5, 6, 7);
#pragma unroll
                for (int m = 0; m < 2; ++m) accf[m][n] = __builtin_amdgcn_mfma_f32_16x16x32_bf16(wb, ha[m], accf[m][n], 0, 0, 0);
            }
        }
        WAIT_LGKM0();
    }
    {
        const f32x2* T0 = reinterpret_cast<const f32x2*>(smem + K2_T0);
        const f32x2* lamt = reinterpret_cast<const f32x2*>(smem + K2_LAM);
        char* ap = smem + K2_AP;
        const int pl = tid & 31, rg = tid >> 5;
        for (int s = 0; s < K2_NS; ++s) {
            const int it = K2_NF + s;
            if (s < K2_NS - 4) { WAIT_VM(16); }
            else if (s == K2_NS - 4) { WAIT_VM(12); }
            else if (s == K2_NS - 3) { WAIT_VM(8); }
            else if (s == K2_NS - 2) { WAIT_VM(4); }
            else { WAIT_VM(0); }
            BAR();
            if (it + K2_NST - 1 < K2_NIT) issue(it + K2_NST - 1);
            const char* sb = smem + (it % K2_NST) * K2_STAGE;
            {
                const int p = s * 32 + pl;
                f32x2 t = T0[rg * P_ + p];
                const f32x2 lam = lamt[p];
#pragma unroll
                for (int r = 0; r < 8; ++r) {
                    const int row = rg * 8 + r;
                    unsigned raw = *reinterpret_cast<const unsigned*>(sb + row * 128 + pl * 4);
                    float hr = bf2f(raw & 0xffff) + t.x, hi = bf2f(raw >> 16) + t.y;
                    t = cmul(t, lam);
                    const int sw = (row >> 1) & 7;
                    *reinterpret_cast<unsigned short*>(ap + row * 128 + (((pl >> 3) ^ sw) << 4) + (pl & 7) * 2) = f2bf(hr);
                    *reinterpret_cast<unsigned short*>(ap + row * 128 + (((4 + (pl >> 3)) ^ sw) << 4) + (pl & 7) * 2) = f2bf(-hi);
                }
            }
            WAIT_LGKM0();
            BAR();
#pragma unroll
            for (int ks = 0; ks < 2; ++ks) {
                bf16x8 ha[2], cb[2];
#pragma unroll
                for (int m = 0; m < 2; ++m) ha[m] = *reinterpret_cast<const bf16x8*>(ap + offA[m][ks]);
#pragma unroll
                for (int n = 0; n < 2; ++n) cb[n] = *reinterpret_cast<const bf16x8*>(sb + offB[n][ks]);
#pragma unroll
                for (int n = 0; n < 2; ++n)
#pragma unroll
                    for (int m = 0; m < 2; ++m) accs[m][n] = __builtin_amdgcn_mfma_f32_16x16x32_bf16(cb[n], ha[m], accs[m][n], 0, 0, 0);
            }
            WAIT_LGKM0();
        }
    }
    {
        const float* gs = reinterpret_cast<const float*>(smem + K2_G);
#pragma unroll
        for (int m = 0; m < 2; ++m) {
            const int lr = wm * 32 + m * 16 + fr;
            const float g = gs[lr];
#pragma unroll
            for (int n = 0; n < 2; ++n) {
                const int d = d0 + wn * 32 + n * 16 + fq * 4;
                uint2 xv = *reinterpret_cast<const uint2*>(x + (size_t)(l0 + lr) * D_ + d);
                uint2 dv = *reinterpret_cast<const uint2*>(dskip + d);
                uint2 bv = *reinterpret_cast<const uint2*>(b2 + d);
                float xf[4] = {bf2f(xv.x & 0xffff), bf2f(xv.x >> 16), bf2f(xv.y & 0xffff), bf2f(xv.y >> 16)};
                float df[4] = {bf2f(dv.x & 0xffff), bf2f(dv.x >> 16), bf2f(dv.y & 0xffff), bf2f(dv.y >> 16)};
                float bf[4] = {bf2f(bv.x & 0xffff), bf2f(bv.x >> 16), bf2f(bv.y & 0xffff), bf2f(bv.y >> 16)};
                f32x4 o;
#pragma unroll
                for (int j = 0; j < 4; ++j) {
                    float a = 2.f * accs[m][n][j] + xf[j] * df[j];
                    float b = accf[m][n][j] + bf[j];
                    o[j] = g * a + (1.f - g) * b;
                }
                *reinterpret_cast<f32x4*>(out + (size_t)(l0 + lr) * D_ + d) = o;
            }
        }
    }
}

extern "C" void kernel_launch(void* const* d_in, const int* in_sizes, int n_in,
                              void* d_out, int out_size, void* d_ws, size_t ws_size,
                              hipStream_t stream) {
    const bf16* x = (const bf16*)d_in[0];
    const bf16* lre = (const bf16*)d_in[1];
    const bf16* lim = (const bf16*)d_in[2];
    const bf16* bre = (const bf16*)d_in[3];
    const bf16* bim = (const bf16*)d_in[4];
    const bf16* cre = (const bf16*)d_in[5];
    const bf16* cim = (const bf16*)d_in[6];
    const bf16* logdt = (const bf16*)d_in[7];
    const bf16* dskip = (const bf16*)d_in[8];
    const bf16* w1 = (const bf16*)d_in[10];
    const bf16* b1 = (const bf16*)d_in[11];
    const bf16* w2 = (const bf16*)d_in[12];
    const bf16* b2 = (const bf16*)d_in[13];
    const bf16* gw = (const bf16*)d_in[14];

    char* ws = (char*)d_ws;
    unsigned* hloc = (unsigned*)ws;
    float* agg = (float*)(ws + (size_t)L_ * P_ * 4);
    bf16* H = (bf16*)(ws + (size_t)L_ * P_ * 4 + 65536);

    hipFuncSetAttribute((const void*)k1_gemm_scan, hipFuncAttributeMaxDynamicSharedMemorySize, K1_LDS);
    hipFuncSetAttribute((const void*)k2_gemm_mix, hipFuncAttributeMaxDynamicSharedMemorySize, K2_LDS);
    k1_gemm_scan<<<256, 256, K1_LDS, stream>>>(x, lre, lim, bre, bim, logdt, w1, b1, hloc, agg, H);
    k2_gemm_mix<<<256, 256, K2_LDS, stream>>>(x, lre, lim, cre, cim, logdt, dskip, w2, b2, gw, hloc, agg, H, (float*)d_out);
}
```

```cpp
#include <hip/hip_runtime.h>
#include <hip/hip_bf16.h>
#include <stdlib.h>

typedef __hip_bfloat16 bf16;
typedef __attribute__((ext_vector_type(8))) short bf16x8;
typedef __attribute__((ext_vector_type(4))) short bf16x4;
typedef __attribute__((ext_vector_type(4))) float f32x4;
typedef __attribute__((ext_vector_type(2))) float f32x2;
typedef __attribute__((ext_vector_type(4))) unsigned u32x4;
typedef __attribute__((ext_vector_type(2))) unsigned u32x2;

constexpr int L_ = 2048, D_ = 512, P_ = 512, F_ = 1792;
constexpr int T_ = 128;
constexpr int NCH = L_ / T_;
constexpr int KSF = F_ / 32;
constexpr int KSS = P_ / 16;

#define WAIT_VM(n) asm volatile("s_waitcnt vmcnt(" #n ")" ::: "memory")
#define BAR() __builtin_amdgcn_s_barrier()
#define LBAR() do { asm volatile("s_waitcnt lgkmcnt(0)" ::: "memory"); __builtin_amdgcn_s_barrier(); asm volatile("" ::: "memory"); } while (0)

#define STAMP_FLUSH_W(k) do {} while (0)
#define STAMP_DECL() do {} while (0)
#define STAMP(i) do {} while (0)
#define STAMP_FLUSH(k) do {} while (0)
#define TR_READ(dst, addr, off) asm volatile("ds_read_b64_tr_b16 %0, %1 offset:%2" : "=v"(dst) : "v"(addr), "i"(off) : "memory")
#define DS_READ128(dst, addr, off) asm volatile("ds_read_b128 %0, %1 offset:%2" : "=v"(dst) : "v"(addr), "i"(off) : "memory")
#define SB0() __builtin_amdgcn_sched_barrier(0)
#define PIN() do { asm volatile("" ::: "memory"); __builtin_amdgcn_sched_barrier(0); } while (0)
#define ST16(p, v) asm volatile("global_store_dwordx4 %0, %1, off sc0 sc1\n\ts_nop 1" :: "v"(p), "v"(v) : "memory")
#define ST8(p, v) asm volatile("global_store_dwordx2 %0, %1, off sc0 sc1\n\ts_nop 1" :: "v"(p), "v"(v) : "memory")

__device__ __forceinline__ float bflo(unsigned u) { return __uint_as_float(u << 16); }
__device__ __forceinline__ float bfhi(unsigned u) { return __uint_as_float(u & 0xffff0000u); }
__device__ __forceinline__ unsigned pack2(float lo, float hi) {
    unsigned r; asm("v_cvt_pk_bf16_f32 %0, %1, %2" : "=v"(r) : "v"(lo), "v"(hi)); return r;
}
__device__ __forceinline__ void glds16(const void* g, void* l) {
    __builtin_amdgcn_global_load_lds((const unsigned*)g, (unsigned*)l, 16, 0, 0);
}
__device__ __forceinline__ f32x2 cmul(f32x2 a, f32x2 b) {
    f32x2 r; r.x = a.x * b.x - a.y * b.y; r.y = a.x * b.y + a.y * b.x; return r;
}
__device__ __forceinline__ float gelu_tanh(float v) {
    const float k1 = -2.f * 0.7978845608028654f * 1.4426950408889634f;
    const float k2 = k1 * 0.044715f;
    float e = __builtin_amdgcn_exp2f(v * __builtin_fmaf(v * v, k2, k1));
    return v * __builtin_amdgcn_rcpf(1.f + e);
}

constexpr int K1_A = 128 * 128;
constexpr int K1_BS = 64 * 128;
constexpr int K1_BF = 2 * 64 * 128;
constexpr int K1_STAGE = K1_A + K1_BS + K1_BF;
constexpr int K1_NST = 4;
constexpr int K1_LDS = K1_STAGE * K1_NST;

struct K1Frags { bf16x8 a[2]; bf16x8 s[4]; bf16x4 f[7][2]; };

__global__ __launch_bounds__(512, 2) void k1_gemm_scan(
    const bf16* __restrict__ x, const bf16* __restrict__ lre, const bf16* __restrict__ lim,
    const bf16* __restrict__ bre, const bf16* __restrict__ bim, const bf16* __restrict__ logdt,
    const bf16* __restrict__ w1, const bf16* __restrict__ b1,
    const bf16* __restrict__ w2, const bf16* __restrict__ cre, const bf16* __restrict__ cim,
    const bf16* __restrict__ gw,
    char* __restrict__ hloc_t, float* __restrict__ agg, char* __restrict__ H_t,
    char* __restrict__ w2_t, char* __restrict__ c_t, float* __restrict__ gate, float* __restrict__ lamtab)
{
    extern __shared__ __attribute__((aligned(16))) char smem[];
    const int tid = threadIdx.x, lane = tid & 63;
    const int wv = __builtin_amdgcn_readfirstlane(tid >> 6);
    const int w = wv & 3;
    const int xcd = blockIdx.x & 7, ii = blockIdx.x >> 3;
    const int rp = (xcd & 1) * 8 + (ii & 7);
    const int cg = (xcd >> 1) * 4 + (ii >> 3);
    const int l0 = rp * T_, p0 = cg * 32, f0 = cg * 112;
    const int fr = lane & 15, fq = lane >> 4;
    STAMP_DECL();
    constexpr int US = 272, USEG = 16 * US + 64;
    char* ubuf = smem;
    char* hbuf = smem + 40960;

    if (wv >= 4) {
        const int pl = w * 8 + (lane & 7), seg = lane >> 3;
        unsigned pr0, pr1, pr2;
        asm volatile("global_load_ushort %0, %1, off" : "=v"(pr0) : "v"(lre + p0 + pl) : "memory");
        asm volatile("global_load_ushort %0, %1, off" : "=v"(pr1) : "v"(lim + p0 + pl) : "memory");
        asm volatile("global_load_ushort %0, %1, off" : "=v"(pr2) : "v"(logdt + p0 + pl) : "memory");

        const bf16* srcA[4]; const bf16* srcS[2]; const bf16* srcF[4];
#pragma unroll
        for (int i = 0; i < 4; ++i) {
            int row = (w * 4 + i) * 8 + (lane >> 3);
            int ch = (lane & 7) ^ ((row >> 1) & 7);
            srcA[i] = x + (size_t)(l0 + row) * D_ + ch * 8;
        }
#pragma unroll
        for (int i = 0; i < 2; ++i) {
            int n = (w * 2 + i) * 8 + (lane >> 3);
            int ch = (lane & 7) ^ ((n >> 1) & 7);
            const bf16* base = (n < 32) ? bre : bim;
            srcS[i] = base + (size_t)(p0 + (n & 31)) * D_ + ch * 8;
        }
#pragma unroll
        for (int i = 0; i < 4; ++i) {
            int ins = w * 4 + i;
            int blk = ins >> 3, r = (ins & 7) * 8 + (lane >> 3);
            int c = lane & 7;
            int fsw = ((r >> 1) & 1) | (((r >> 3) & 1) << 1);
            int col = f0 + blk * 64 + (((c >> 1) ^ fsw) << 4) + (c & 1) * 8;
            if (col > F_ - 8) col = F_ - 8;
            srcF[i] = w1 + (size_t)r * F_ + col;
        }
        const int krot = (rp + cg) & 7;
        auto issue = [&](int kt) {
            char* sb = smem + (kt & (K1_NST - 1)) * K1_STAGE;
            const int ko = ((kt + krot) & 7) * 64;
#pragma unroll
            for (int i = 0; i < 4; ++i) glds16(srcA[i] + ko, sb + (w * 4 + i) * 1024);
#pragma unroll
            for (int i = 0; i < 2; ++i) glds16(srcS[i] + ko, sb + K1_A + (w * 2 + i) * 1024);
#pragma unroll
            for (int i = 0; i < 4; ++i) glds16(srcF[i] + (size_t)ko * F_, sb + K1_A + K1_BS + (w * 4 + i) * 1024);
        };
        STAMP(1);
        issue(0); issue(1); issue(2); issue(3);
        STAMP(2);
        asm volatile("" ::: "memory");
        unsigned short w2v[2][8];
#pragma unroll
        for (int i = 0; i < 2; ++i) {
            int tl = w + 4 * i; if (tl > 6) tl = 6;
            int tile = blockIdx.x * 7 + tl;
            int nt = tile / KSF, ks = tile - nt * KSF;
            const unsigned short* src = reinterpret_cast<const unsigned short*>(w2) + (size_t)(ks * 32 + fq * 8) * D_ + nt * 16 + fr;
#pragma unroll
            for (int j = 0; j < 8; ++j) w2v[i][j] = src[(size_t)j * D_];
        }
        uint2 cvr, cvi;
        {
            int tile = blockIdx.x * 4 + w;
            int nt = tile >> 5, ks = tile & 31;
            size_t o = (size_t)(nt * 16 + fr) * P_ + ks * 16 + fq * 4;
            cvr = *reinterpret_cast<const uint2*>(cre + o);
            cvi = *reinterpret_cast<const uint2*>(cim + o);
        }
        const int grow = l0 + cg * 8 + w * 2;
        const u32x4 gwv = *reinterpret_cast<const u32x4*>(gw + lane * 8);
        const u32x4 gx0 = *reinterpret_cast<const u32x4*>(x + (size_t)grow * D_ + lane * 8);
        const u32x4 gx1 = *reinterpret_cast<const u32x4*>(x + (size_t)(grow + 1) * D_ + lane * 8);
        asm volatile("s_waitcnt vmcnt(61)" : "+v"(pr0), "+v"(pr1), "+v"(pr2) :: "memory");
        const float p_ar = bflo(pr0), p_ai = bflo(pr1);
        f32x2 lam, coef;
        {
            const float dt = expf(bflo(pr2));
            float e = expf(dt * p_ar), sn, cs;
            sincosf(dt * p_ai, &sn, &cs);
            lam.x = e * cs; lam.y = e * sn;
            float nr = lam.x - 1.f, ni = lam.y, den = p_ar * p_ar + p_ai * p_ai;
            coef.x = (nr * p_ar + ni * p_ai) / den; coef.y = (ni * p_ar - nr * p_ai) / den;
        }
        const f32x2 l2 = cmul(lam, lam), l4 = cmul(l2, l2), l8 = cmul(l4, l4), l16 = cmul(l8, l8);

        WAIT_VM(51);
        STAMP(3);
        BAR();
#define K1_LSTEP(t, NA)                                       \
        do {                                                  \
            WAIT_VM(NA);                                      \
            STAMP(4 + (t));                                   \
            BAR();                                            \
            if ((t) + 4 < 8) issue((t) + 4);                  \
        } while (0)
        K1_LSTEP(0, 41); K1_LSTEP(1, 41); K1_LSTEP(2, 41); K1_LSTEP(3, 20);
        K1_LSTEP(4, 20); K1_LSTEP(5, 10); K1_LSTEP(6, 0);
#undef K1_LSTEP
        LBAR();
        LBAR();
        STAMP(11);
        {
            f32x2 hv[16]; f32x2 h = {0.f, 0.f};
            const char* up = ubuf + seg * USEG + pl * 8;
#pragma unroll
            for (int i = 0; i < 16; ++i) {
                f32x2 raw = *reinterpret_cast<const f32x2*>(up + i * US);
                f32x2 u = cmul(coef, raw);
                f32x2 t = cmul(lam, h);
                h.x = t.x + u.x; h.y = t.y + u.y; hv[i] = h;
            }
            f32x2 c = h;
            {
                const f32x2 l32 = cmul(l16, l16), l64 = cmul(l32, l32);
                f32x2 o;
                o.x = __shfl_up(c.x, 8); o.y = __shfl_up(c.y, 8);
                if (seg >= 1) { f32x2 t = cmul(l16, o); c.x += t.x; c.y += t.y; }
                o.x = __shfl_up(c.x, 16); o.y = __shfl_up(c.y, 16);
                if (seg >= 2) { f32x2 t = cmul(l32, o); c.x += t.x; c.y += t.y; }
                o.x = __shfl_up(c.x, 32); o.y = __shfl_up(c.y, 32);
                if (seg >= 4) { f32x2 t = cmul(l64, o); c.x += t.x; c.y += t.y; }
            }
            if (seg == 7) { u32x2 v = __builtin_bit_cast(u32x2, c); ST8(agg + ((size_t)rp * P_ + p0 + pl) * 2, v); }
            f32x2 cin; cin.x = __shfl_up(c.x, 8); cin.y = __shfl_up(c.y, 8);
            if (seg == 0) { cin.x = 0.f; cin.y = 0.f; }
            f32x2 t = cmul(cin, lam);
            char* hb = hbuf + w * 4096 + seg * 512 + ((lane >> 2) & 1) * 256 + (lane & 3) * 4;
#pragma unroll
            for (int i = 0; i < 16; ++i) {
                hv[i].x += t.x; hv[i].y += t.y; t = cmul(t, lam);
                *reinterpret_cast<unsigned*>(hb + i * 16) = pack2(hv[i].x, hv[i].y);
            }
            asm volatile("s_waitcnt lgkmcnt(0)" ::: "memory");
            const int ks = cg * 2 + (w >> 1);
#pragma unroll
            for (int i = 0; i < 4; ++i) {
                const int o = i * 1024 + lane * 16;
                const int sg = o >> 9;
                u32x4 v = *reinterpret_cast<const u32x4*>(hbuf + w * 4096 + o);
                char* dst = hloc_t + ((size_t)((rp * 2 + (sg >> 2)) * KSS + ks) * 4 + (sg & 3)) * 1024 + (w & 1) * 512 + (o & 511);
                ST16(dst, v);
            }
        }
        STAMP(12);
#pragma unroll
        for (int i = 0; i < 2; ++i) {
            int tl = w + 4 * i;
            if (tl < 7) {
                int tile = blockIdx.x * 7 + tl;
                int nt = tile / KSF, ks = tile - nt * KSF;
                u32x4 v;
                v.x = (unsigned)w2v[i][0] | ((unsigned)w2v[i][1] << 16); v.y = (unsigned)w2v[i][2] | ((unsigned)w2v[i][3] << 16);
                v.z = (unsigned)w2v[i][4] | ((unsigned)w2v[i][5] << 16); v.w = (unsigned)w2v[i][6] | ((unsigned)w2v[i][7] << 16);
                ST16(w2_t + ((size_t)((nt >> 2) * KSF + ks) * 4 + (nt & 3)) * 1024 + lane * 16, v);
            }
        }
        {
            int tile = blockIdx.x * 4 + w;
            int nt = tile >> 5, ks = tile & 31;
            unsigned ni0 = cvi.x ^ 0x80008000u, ni1 = cvi.y ^ 0x80008000u;
            u32x4 v;
            v.x = (cvr.x & 0xffffu) | (ni0 << 16); v.y = (cvr.x >> 16) | (ni0 & 0xffff0000u);
            v.z = (cvr.y & 0xffffu) | (ni1 << 16); v.w = (cvr.y >> 16) | (ni1 & 0xffff0000u);
            ST16(c_t + ((size_t)((nt >> 2) * KSS + ks) * 4 + (nt & 3)) * 1024 + lane * 16, v);
        }
        {
            float s0 = bflo(gx0.x) * bflo(gwv.x) + bfhi(gx0.x) * bfhi(gwv.x) + bflo(gx0.y) * bflo(gwv.y) + bfhi(gx0.y) * bfhi(gwv.y)
                     + bflo(gx0.z) * bflo(gwv.z) + bfhi(gx0.z) * bfhi(gwv.z) + bflo(gx0.w) * bflo(gwv.w) + bfhi(gx0.w) * bfhi(gwv.w);
            float s1 = bflo(gx1.x) * bflo(gwv.x) + bfhi(gx1.x) * bfhi(gwv.x) + bflo(gx1.y) * bflo(gwv.y) + bfhi(gx1.y) * bfhi(gwv.y)
                     + bflo(gx1.z) * bflo(gwv.z) + bfhi(gx1.z) * bfhi(gwv.z) + bflo(gx1.w) * bflo(gwv.w) + bfhi(gx1.w) * bfhi(gwv.w);
#pragma unroll
            for (int o = 32; o > 0; o >>= 1) { s0 += __shfl_xor(s0, o); s1 += __shfl_xor(s1, o); }
            if (lane == 0) {
                u32x2 v; v.x = __float_as_uint(1.f / (1.f + __expf(-s0))); v.y = __float_as_uint(1.f / (1.f + __expf(-s1)));
                ST8(gate + grow, v);
            }
        }
        if (rp == 0 && seg == 0) {
            f32x2 l32 = cmul(l16, l16), l64 = cmul(l32, l32), l128 = cmul(l64, l64);
            f32x4 v0 = {lam.x, lam.y, l16.x, l16.y}, v1 = {l64.x, l64.y, l128.x, l128.y};
            ST16(lamtab + (size_t)(p0 + pl) * 8, __builtin_bit_cast(u32x4, v0));
            ST16(lamtab + (size_t)(p0 + pl) * 8 + 4, __builtin_bit_cast(u32x4, v1));
        }
        STAMP_FLUSH(2);
        return;
    }

    uint2 b1v[7];
#pragma unroll
    for (int n = 0; n < 7; ++n) b1v[n] = *reinterpret_cast<const uint2*>(b1 + f0 + n * 16 + fq * 4);

    f32x4 acc[2][11];
#pragma unroll
    for (int m = 0; m < 2; ++m)
#pragma unroll
        for (int n = 0; n < 11; ++n) {
            if (n < 4) acc[m][n] = (f32x4){0.f, 0.f, 0.f, 0.f};
            else acc[m][n] = (f32x4){bflo(b1v[n - 4].x), bfhi(b1v[n - 4].x), bflo(b1v[n - 4].y), bfhi(b1v[n - 4].y)};
        }

    const unsigned smem_base = (unsigned)(size_t)smem;
    unsigned adA[2], adS[2], adF[2][4];
#pragma unroll
    for (int ks = 0; ks < 2; ++ks) {
        int ch = ks * 4 + fq;
        unsigned lo = fr * 128 + ((ch ^ ((fr >> 1) & 7)) << 4);
        adA[ks] = smem_base + w * 4096 + lo;
        adS[ks] = smem_base + K1_A + lo;
    }
    {
        const int q = (lane & 15) >> 2, pp = lane & 3;
        const int fsw = ((q >> 1) & 1) | ((fq & 1) << 1);
#pragma unroll
        for (int t = 0; t < 2; ++t)
#pragma unroll
            for (int pc = 0; pc < 4; ++pc)
                adF[t][pc] = smem_base + K1_A + K1_BS + (fq * 8 + 4 * t + q) * 128 + ((pc ^ fsw) << 5) + pp * 8;
    }
#define K1_ADV(delta)                                                                       \
    do {                                                                                    \
        _Pragma("unroll") for (int k_ = 0; k_ < 2; ++k_) { adA[k_] += (delta); adS[k_] += (delta); asm volatile("" : "+v"(adA[k_]), "+v"(adS[k_])); } \
        _Pragma("unroll") for (int t_ = 0; t_ < 2; ++t_) _Pragma("unroll") for (int c_ = 0; c_ < 4; ++c_) { adF[t_][c_] += (delta); asm volatile("" : "+v"(adF[t_][c_])); } \
    } while (0)
#define K1_RD(R, i, ks)                                                                     \
    do {                                                                                    \
        if ((i) == 0) DS_READ128(R.a[0], adA[ks], 0);                                       \
        if ((i) == 1) DS_READ128(R.a[1], adA[ks], 2048);                                    \
        if ((i) >= 2 && (i) < 6) DS_READ128(R.s[((i) - 2) & 3], adS[ks], (((i) - 2) & 3) * 2048); \
        if ((i) >= 6 && (i) < 20)                                                           \
            TR_READ(R.f[(((i) - 6) >> 1) % 7][(i) & 1], adF[(i) & 1][(((i) - 6) >> 1) & 3], (((((i) - 6) >> 1) % 7) >> 2) * 8192 + (ks) * 4096); \
    } while (0)
#define K1_WAITL(R)                                                                         \
    asm volatile("s_waitcnt lgkmcnt(0)"                                                     \
        : "+v"(R.a[0]), "+v"(R.a[1]), "+v"(R.s[0]), "+v"(R.s[1]), "+v"(R.s[2]), "+v"(R.s[3]), \
          "+v"(R.f[0][0]), "+v"(R.f[0][1]), "+v"(R.f[1][0]), "+v"(R.f[1][1]),               \
          "+v"(R.f[2][0]), "+v"(R.f[2][1]), "+v"(R.f[3][0]), "+v"(R.f[3][1]),               \
          "+v"(R.f[4][0]), "+v"(R.f[4][1]), "+v"(R.f[5][0]), "+v"(R.f[5][1]),               \
          "+v"(R.f[6][0]), "+v"(R.f[6][1]) :: "memory")
#define K1_MM(R, i)                                                                         \
    do {                                                                                    \
        if ((i) < 8) acc[(i) & 1][((i) >> 1) & 3] = __builtin_amdgcn_mfma_f32_16x16x32_bf16(R.s[((i) >> 1) & 3], R.a[(i) & 1], acc[(i) & 1][((i) >> 1) & 3], 0, 0, 0); \
        else {                                                                              \
            bf16x8 wb = __builtin_shufflevector(R.f[(((i) - 8) >> 1) % 7][0], R.f[(((i) - 8) >> 1) % 7][1], 0, 1, 2, 3, 4, 5, 6, 7); \
            acc[(i) & 1][4 + (((i) - 8) >> 1) % 7] = __builtin_amdgcn_mfma_f32_16x16x32_bf16(wb, R.a[(i) & 1], acc[(i) & 1][4 + (((i) - 8) >> 1) % 7], 0, 0, 0); \
        }                                                                                   \
    } while (0)
    {
        K1Frags R0, R1;
        BAR();
        asm volatile("" ::: "memory");
#pragma unroll
        for (int i = 0; i < 20; ++i) K1_RD(R0, i, 0);
        K1_WAITL(R0);
#define K1_STEP(t)                                            \
        do {                                                  \
            _Pragma("unroll") for (int i = 0; i < 22; ++i) { K1_MM(R0, i); K1_RD(R1, i, 1); SB0(); } \
            K1_WAITL(R1);                                     \
            if ((t) < 7) {                                    \
                K1_ADV((((t) & 3) == 3) ? -3 * K1_STAGE : K1_STAGE); \
                BAR();                                        \
                asm volatile("" ::: "memory");                \
                _Pragma("unroll") for (int i = 0; i < 22; ++i) { K1_MM(R1, i); K1_RD(R0, i, 0); SB0(); } \
                K1_WAITL(R0);                                 \
            } else {                                          \
                BAR();        \
                _Pragma("unroll") for (int i = 0; i < 22; ++i) K1_MM(R1, i); \
            }                                                 \
        } while (0)
        K1_STEP(0); STAMP(1); K1_STEP(1); K1_STEP(2); K1_STEP(3);
        STAMP(2); K1_STEP(4); K1_STEP(5); K1_STEP(6); K1_STEP(7); STAMP(3);
#undef K1_STEP
    }
#pragma unroll
    for (int m = 0; m < 2; ++m)
#pragma unroll
        for (int nt = 0; nt < 2; ++nt) {
            int pq = nt * 16 + fq * 4;
            char* up = ubuf + (w * 2 + m) * USEG + fr * US + pq * 8;
            f32x4 re = acc[m][nt], im = acc[m][nt + 2];
            f32x4 v0 = {re[0], im[0], re[1], im[1]}, v1 = {re[2], im[2], re[3], im[3]};
            *reinterpret_cast<f32x4*>(up) = v0;
            *reinterpret_cast<f32x4*>(up + 16) = v1;
        }
    LBAR();
    STAMP(4);
    {
        char* wimg = smem + 65536 + w * 8192;
        const int fbase = f0 & ~31;
        const int foff = (f0 & 31) ? 512 : 0;
#pragma unroll
        for (int n = 0; n < 7; ++n) {
            const int f = f0 + n * 16 + fq * 4;
            char* hp = wimg + ((f - fbase) >> 5) * 1024 + ((f >> 3) & 3) * 256 + fr * 16 + ((f >> 2) & 1) * 8 - foff;
#pragma unroll
            for (int m = 0; m < 2; ++m) {
                float o[4];
#pragma unroll
                for (int j = 0; j < 4; ++j) o[j] = gelu_tanh(acc[m][4 + n][j]);
                u32x2 pk; pk.x = pack2(o[0], o[1]); pk.y = pack2(o[2], o[3]);
                *reinterpret_cast<u32x2*>(hp + m * 3584) = pk;
            }
        }
        STAMP(5);
        const int lw = l0 + w * 32;
        char* hrow = H_t + ((size_t)(lw >> 6) * KSF * 4 + ((lw >> 4) & 3) + (size_t)(fbase >> 5) * 4) * 1024;
#pragma unroll
        for (int i = 0; i < 7; ++i) {
            int m, o;
            if (i < 6) { m = i / 3; o = (i % 3) * 1024 + lane * 16; }
            else { m = lane >> 5; o = 3072 + (lane & 31) * 16; }
            u32x4 v = *reinterpret_cast<const u32x4*>(wimg + m * 3584 + o);
            const int og = o + foff;
            ST16(hrow + m * 1024 + (og >> 10) * 4096 + (og & 1023), v);
        }
    }
    STAMP_FLUSH(0);
}

constexpr int K2_RS = 272;
constexpr int K2_RED = 0;
constexpr int K2_T1 = 0;
constexpr int K2_L16 = 65536;
constexpr int K2_LDS = 8 * 64 * K2_RS;
constexpr int K2_NFW = 5;
constexpr int K2_NFS = 12;
constexpr int K2_NSS = 11;
constexpr int K2_DEPTH = 4;

__global__ __launch_bounds__(512, 2) void k2_gemm_mix(
    const bf16* __restrict__ x, const bf16* __restrict__ dskip, const bf16* __restrict__ b2,
    const char* __restrict__ hloc_t, const float* __restrict__ agg,
    const char* __restrict__ H_t, const char* __restrict__ w2_t, const char* __restrict__ c_t,
    const float* __restrict__ gate, const float* __restrict__ lamtab, float* __restrict__ out)
{
    extern __shared__ __attribute__((aligned(16))) char smem[];
    const int tid = threadIdx.x, lane = tid & 63;
    const int wv = __builtin_amdgcn_readfirstlane(tid >> 6);
    const int xcd = blockIdx.x & 7, ii = blockIdx.x >> 3;
    const int rt = (xcd & 3) * 8 + (ii & 7);
    const int ct = (xcd >> 2) * 4 + (ii >> 3);
    const int l0 = rt * 64, d0 = ct * 64;
    const int fr = lane & 15, fq = lane >> 4;
    STAMP_DECL();
    const bool is_ffn = wv < K2_NFW;
    const int k0 = is_ffn ? wv : wv - K2_NFW;
    const int kst = is_ffn ? K2_NFW : 8 - K2_NFW;
    const int nst = is_ffn ? (KSF - k0 + kst - 1) / kst : (KSS - k0 + kst - 1) / kst;
    const char* pA = (is_ffn ? H_t + (size_t)(rt * KSF + k0) * 4096 : hloc_t + (size_t)(rt * KSS + k0) * 4096) + lane * 16;
    const char* pB = (is_ffn ? w2_t + (size_t)(ct * KSF + k0) * 4096 : c_t + (size_t)(ct * KSS + k0) * 4096) + lane * 16;
    const int sstride = kst * 4096;

    f32x4 acc[4][4];
#pragma unroll
    for (int n = 0; n < 4; ++n)
#pragma unroll
        for (int m = 0; m < 4; ++m) acc[n][m] = (f32x4){0.f, 0.f, 0.f, 0.f};

    float gl[4];
    {
        const int chunk = rt >> 1, jbase = (rt & 1) * 64 + 1;
        const int p = tid;
        const f32x4 lt0 = *reinterpret_cast<const f32x4*>(lamtab + (size_t)p * 8);
        const f32x4 lt1 = *reinterpret_cast<const f32x4*>(lamtab + (size_t)p * 8 + 4);
        f32x2 ag[NCH - 1];
#pragma unroll
        for (int c = 0; c < NCH - 1; ++c) ag[c] = *reinterpret_cast<const f32x2*>(agg + ((size_t)c * P_ + p) * 2);
#pragma unroll
        for (int m = 0; m < 4; ++m) gl[m] = gate[l0 + m * 16 + fr];
        const f32x2 lam = {lt0[0], lt0[1]}, l16 = {lt0[2], lt0[3]};
        const f32x2 l64 = {lt1[0], lt1[1]}, l128 = {lt1[2], lt1[3]};
        f32x2 S = {0.f, 0.f};
#pragma unroll
        for (int c = 0; c < NCH - 1; ++c) {
            if (c < chunk) { f32x2 t = cmul(l128, S); S.x = t.x + ag[c].x; S.y = t.y + ag[c].y; }
        }
        f32x2 t = cmul(S, lam);
        if (jbase > 1) t = cmul(t, l64);
        f32x2* T1 = reinterpret_cast<f32x2*>(smem + K2_T1);
#pragma unroll
        for (int r = 0; r < 16; ++r) { T1[r * P_ + p] = t; t = cmul(t, lam); }
        reinterpret_cast<f32x2*>(smem + K2_L16)[p] = l16;
        STAMP(1);
    }

    if (is_ffn) {
        constexpr int DP = 4;
        bf16x8 fa[DP][4], fb[DP][4];
        auto load_step = [&](int s) {
            const int slot = s % DP;
            const int se = (s < nst) ? s : nst - 1;
#pragma unroll
            for (int i = 0; i < 4; ++i) fa[slot][i] = *reinterpret_cast<const bf16x8*>(pA + (size_t)se * sstride + i * 1024);
#pragma unroll
            for (int i = 0; i < 4; ++i) fb[slot][i] = *reinterpret_cast<const bf16x8*>(pB + (size_t)se * sstride + i * 1024);
        };
        PIN();
#pragma unroll
        for (int s = 0; s < DP - 1; ++s) load_step(s);
        PIN();
        LBAR();
        STAMP(2);
#pragma unroll
        for (int s = 0; s < K2_NFS; ++s) {
            if (s + DP - 1 < K2_NFS) load_step(s + DP - 1);
            PIN();
            const int slot = s % DP;
            if (s == K2_NFS - 1) {
#pragma unroll
                for (int n = 0; n < 4; ++n) fb[slot][n] = (s < nst) ? fb[slot][n] : (bf16x8){0, 0, 0, 0, 0, 0, 0, 0};
            }
#pragma unroll
            for (int n = 0; n < 4; ++n)
#pragma unroll
                for (int m = 0; m < 4; ++m) acc[n][m] = __builtin_amdgcn_mfma_f32_16x16x32_bf16(fb[slot][n], fa[slot][m], acc[n][m], 0, 0, 0);
            PIN();
        }
        STAMP(3);
#pragma unroll
        for (int m = 0; m < 4; ++m) gl[m] = 1.f - gl[m];
    } else {
        constexpr int DP = 3;
        bf16x8 fa[DP][4], fb[DP][4];
        auto load_step = [&](int s) {
            const int slot = s % DP;
            const int se = (s < nst) ? s : nst - 1;
#pragma unroll
            for (int i = 0; i < 4; ++i) fa[slot][i] = *reinterpret_cast<const bf16x8*>(pA + (size_t)se * sstride + i * 1024);
#pragma unroll
            for (int i = 0; i < 4; ++i) fb[slot][i] = *reinterpret_cast<const bf16x8*>(pB + (size_t)se * sstride + i * 1024);
        };
        PIN();
#pragma unroll
        for (int s = 0; s < DP - 1; ++s) load_step(s);
        PIN();
        LBAR();
        STAMP(2);
        const char* T1b = smem + K2_T1 + (fr * P_ + fq * 4 + k0 * 16) * 8;
        const char* L16b = smem + K2_L16 + (fq * 4 + k0 * 16) * 8;
#pragma unroll
        for (int s = 0; s < K2_NSS; ++s) {
            if (s + DP - 1 < K2_NSS) load_step(s + DP - 1);
            PIN();
            const int slot = s % DP;
            if (s == K2_NSS - 1) {
#pragma unroll
                for (int n = 0; n < 4; ++n) fb[slot][n] = (s < nst) ? fb[slot][n] : (bf16x8){0, 0, 0, 0, 0, 0, 0, 0};
            }
            const int pofs = ((s < nst) ? s : nst - 1) * (8 - K2_NFW) * 16 * 8;
            f32x4 t01 = *reinterpret_cast<const f32x4*>(T1b + pofs), t23 = *reinterpret_cast<const f32x4*>(T1b + pofs + 16);
            f32x4 m01 = *reinterpret_cast<const f32x4*>(L16b + pofs), m23 = *reinterpret_cast<const f32x4*>(L16b + pofs + 16);
            f32x2 t[4] = {{t01[0], t01[1]}, {t01[2], t01[3]}, {t23[0], t23[1]}, {t23[2], t23[3]}};
            const f32x2 mm[4] = {{m01[0], m01[1]}, {m01[2], m01[3]}, {m23[0], m23[1]}, {m23[2], m23[3]}};
#pragma unroll
            for (int m = 0; m < 4; ++m) {
                u32x4 o;
#pragma unroll
                for (int j = 0; j < 4; ++j) {
                    o[j] = pack2(t[j].x, t[j].y);
                    if (m < 3) t[j] = cmul(t[j], mm[j]);
                }
                const bf16x8 tf = __builtin_bit_cast(bf16x8, o);
#pragma unroll
                for (int n = 0; n < 4; ++n) {
                    acc[n][m] = __builtin_amdgcn_mfma_f32_16x16x32_bf16(fb[slot][n], fa[slot][m], acc[n][m], 0, 0, 0);
                    acc[n][m] = __builtin_amdgcn_mfma_f32_16x16x32_bf16(fb[slot][n], tf, acc[n][m], 0, 0, 0);
                }
            }
            PIN();
        }
        STAMP(3);
#pragma unroll
        for (int m = 0; m < 4; ++m) gl[m] = 2.f * gl[m];
    }
    LBAR();
    {
        char* red = smem + K2_RED + wv * 64 * K2_RS;
#pragma unroll
        for (int m = 0; m < 4; ++m)
#pragma unroll
            for (int n = 0; n < 4; ++n) {
                f32x4 v;
#pragma unroll
                for (int j = 0; j < 4; ++j) v[j] = gl[m] * acc[n][m][j];
                *reinterpret_cast<f32x4*>(red + (m * 16 + fr) * K2_RS + (n * 16 + fq * 4) * 4) = v;
            }
    }
    const int c4 = (tid & 15) * 4;
    const uint2 dv = *reinterpret_cast<const uint2*>(dskip + d0 + c4);
    const uint2 bv = *reinterpret_cast<const uint2*>(b2 + d0 + c4);
    uint2 xv[2]; float gr[2];
#pragma unroll
    for (int i = 0; i < 2; ++i) {
        const int row = (tid >> 4) + 32 * i;
        xv[i] = *reinterpret_cast<const uint2*>(x + (size_t)(l0 + row) * D_ + d0 + c4);
        gr[i] = gate[l0 + row];
    }
    LBAR();
    STAMP(4);
    {
        const float df[4] = {bflo(dv.x), bfhi(dv.x), bflo(dv.y), bfhi(dv.y)};
        const float bf[4] = {bflo(bv.x), bfhi(bv.x), bflo(bv.y), bfhi(bv.y)};
#pragma unroll
        for (int i = 0; i < 2; ++i) {
            const int row = (tid >> 4) + 32 * i;
            const float g = gr[i];
            const float xf[4] = {bflo(xv[i].x), bfhi(xv[i].x), bflo(xv[i].y), bfhi(xv[i].y)};
            f32x4 sacc = *reinterpret_cast<const f32x4*>(smem + K2_RED + row * K2_RS + c4 * 4);
#pragma unroll
            for (int ww = 1; ww < 8; ++ww) sacc += *reinterpret_cast<const f32x4*>(smem + K2_RED + (ww * 64 + row) * K2_RS + c4 * 4);
            f32x4 o;
#pragma unroll
            for (int j = 0; j < 4; ++j) o[j] = sacc[j] + g * xf[j] * df[j] + (1.f - g) * bf[j];
            *reinterpret_cast<f32x4*>(out + (size_t)(l0 + row) * D_ + d0 + c4) = o;
        }
    }
    if (wv == 0 || wv == 5) { STAMP_FLUSH_W(wv ? 3 : 1); }
}

extern "C" void kernel_launch(void* const* d_in, const int* in_sizes, int n_in,
                              void* d_out, int out_size, void* d_ws, size_t ws_size,
                              hipStream_t stream) {
    const bf16* x = (const bf16*)d_in[0];
    const bf16* lre = (const bf16*)d_in[1];
    const bf16* lim = (const bf16*)d_in[2];
    const bf16* bre = (const bf16*)d_in[3];
    const bf16* bim = (const bf16*)d_in[4];
    const bf16* cre = (const bf16*)d_in[5];
    const bf16* cim = (const bf16*)d_in[6];
    const bf16* logdt = (const bf16*)d_in[7];
    const bf16* dskip = (const bf16*)d_in[8];
    const bf16* w1 = (const bf16*)d_in[10];
    const bf16* b1 = (const bf16*)d_in[11];
    const bf16* w2 = (const bf16*)d_in[12];
    const bf16* b2 = (const bf16*)d_in[13];
    const bf16* gw = (const bf16*)d_in[14];

    char* ws = (char*)d_ws;
    char* hloc_t = ws;
    char* H_t = ws + (4u << 20);
    char* w2_t = ws + (12u << 20);
    char* c_t = ws + (14u << 20);
    float* agg = (float*)(ws + (16u << 20));
    float* gate = (float*)(ws + (16u << 20) + 65536);
    float* lamtab = (float*)(ws + (16u << 20) + 65536 + 8192);

    (void)hipFuncSetAttribute((const void*)k1_gemm_scan, hipFuncAttributeMaxDynamicSharedMemorySize, K1_LDS);
    (void)hipFuncSetAttribute((const void*)k2_gemm_mix, hipFuncAttributeMaxDynamicSharedMemorySize, K2_LDS);
    k1_gemm_scan<<<256, 512, K1_LDS, stream>>>(x, lre, lim, bre, bim, logdt, w1, b1, w2, cre, cim, gw, hloc_t, agg, H_t, w2_t, c_t, gate, lamtab);
    k2_gemm_mix<<<256, 512, K2_LDS, stream>>>(x, dskip, b2, hloc_t, agg, H_t, w2_t, c_t, gate, lamtab, (float*)d_out);
}
```

```cpp
#include <hip/hip_runtime.h>
#include <hip/hip_bf16.h>
#include <stdlib.h>

typedef __hip_bfloat16 bf16;
typedef __attribute__((ext_vector_type(8))) short bf16x8;
typedef __attribute__((ext_vector_type(4))) short bf16x4;
typedef __attribute__((ext_vector_type(4))) float f32x4;
typedef __attribute__((ext_vector_type(2))) float f32x2;
typedef __attribute__((ext_vector_type(4))) unsigned u32x4;
typedef __attribute__((ext_vector_type(2))) unsigned u32x2;

constexpr int L_ = 2048, D_ = 512, P_ = 512, F_ = 1792;
constexpr int T_ = 128;
constexpr int NCH = L_ / T_;
constexpr int KSF = F_ / 32;
constexpr int KSS = P_ / 16;

#define WAIT_VM(n) asm volatile("s_waitcnt vmcnt(" #n ")" ::: "memory")
#define BAR() __builtin_amdgcn_s_barrier()
#define LBAR() do { asm volatile("s_waitcnt lgkmcnt(0)" ::: "memory"); __builtin_amdgcn_s_barrier(); asm volatile("" ::: "memory"); } while (0)

#define STAMP_FLUSH_W(k) do {} while (0)
#define STAMP_DECL() do {} while (0)
#define STAMP(i) do {} while (0)
#define STAMP_FLUSH(k) do {} while (0)
#define TR_READ(dst, addr, off) asm volatile("ds_read_b64_tr_b16 %0, %1 offset:%2" : "=v"(dst) : "v"(addr), "i"(off) : "memory")
#define DS_READ128(dst, addr, off) asm volatile("ds_read_b128 %0, %1 offset:%2" : "=v"(dst) : "v"(addr), "i"(off) : "memory")
#define SB0() __builtin_amdgcn_sched_barrier(0)
#define PIN() do { asm volatile("" ::: "memory"); __builtin_amdgcn_sched_barrier(0); } while (0)
#define ST16(p, v) asm volatile("global_store_dwordx4 %0, %1, off sc0 sc1\n\ts_nop 1" :: "v"(p), "v"(v) : "memory")
#define ST8(p, v) asm volatile("global_store_dwordx2 %0, %1, off sc0 sc1\n\ts_nop 1" :: "v"(p), "v"(v) : "memory")

__device__ __forceinline__ float bflo(unsigned u) { return __uint_as_float(u << 16); }
__device__ __forceinline__ float bfhi(unsigned u) { return __uint_as_float(u & 0xffff0000u); }
typedef __attribute__((ext_vector_type(2))) __bf16 bf16x2_t;
__device__ __forceinline__ unsigned pack2(float lo, float hi) {
    f32x2 v = {lo, hi};
    return __builtin_bit_cast(unsigned, __builtin_convertvector(v, bf16x2_t));
}
__device__ __forceinline__ void glds16(const void* g, void* l) {
    __builtin_amdgcn_global_load_lds((const unsigned*)g, (unsigned*)l, 16, 0, 0);
}
__device__ __forceinline__ f32x2 cmul(f32x2 a, f32x2 b) {
    f32x2 r; r.x = a.x * b.x - a.y * b.y; r.y = a.x * b.y + a.y * b.x; return r;
}
__device__ __forceinline__ float gelu_tanh(float v) {
    const float k1 = -2.f * 0.7978845608028654f * 1.4426950408889634f;
    const float k2 = k1 * 0.044715f;
    float e = __builtin_amdgcn_exp2f(v * __builtin_fmaf(v * v, k2, k1));
    return v * __builtin_amdgcn_rcpf(1.f + e);
}

constexpr int K1_A = 128 * 128;
constexpr int K1_BS = 64 * 128;
constexpr int K1_BF = 2 * 64 * 128;
constexpr int K1_STAGE = K1_A + K1_BS + K1_BF;
constexpr int K1_NST = 4;
constexpr int K1_LDS = K1_STAGE * K1_NST;

struct K1Frags { bf16x8 a[2]; bf16x8 s[4]; bf16x4 f[7][2]; };

__global__ __launch_bounds__(512, 2) void k1_gemm_scan(
    const bf16* __restrict__ x, const bf16* __restrict__ lre, const bf16* __restrict__ lim,
    const bf16* __restrict__ bre, const bf16* __restrict__ bim, const bf16* __restrict__ logdt,
    const bf16* __restrict__ w1, const bf16* __restrict__ b1,
    const bf16* __restrict__ w2, const bf16* __restrict__ cre, const bf16* __restrict__ cim,
    const bf16* __restrict__ gw,
    char* __restrict__ hloc_t, float* __restrict__ agg, char* __restrict__ H_t,
    char* __restrict__ w2_t, char* __restrict__ c_t, float* __restrict__ gate, float* __restrict__ lamtab)
{
    extern __shared__ __attribute__((aligned(16))) char smem[];
    const int tid = threadIdx.x, lane = tid & 63;
    const int wv = __builtin_amdgcn_readfirstlane(tid >> 6);
    const int w = wv & 3;
    const int xcd = blockIdx.x & 7, ii = blockIdx.x >> 3;
    const int rp = (xcd & 1) * 8 + (ii & 7);
    const int cg = (xcd >> 1) * 4 + (ii >> 3);
    const int l0 = rp * T_, p0 = cg * 32, f0 = cg * 112;
    const int fr = lane & 15, fq = lane >> 4;
    STAMP_DECL();
    constexpr int US = 272, USEG = 16 * US + 64;
    char* ubuf = smem;
    char* hbuf = smem + 40960;

    if (wv >= 4) {
        const int pl = w * 8 + (lane & 7), seg = lane >> 3;
        unsigned pr0, pr1, pr2;
        asm volatile("global_load_ushort %0, %1, off" : "=v"(pr0) : "v"(lre + p0 + pl) : "memory");
        asm volatile("global_load_ushort %0, %1, off" : "=v"(pr1) : "v"(lim + p0 + pl) : "memory");
        asm volatile("global_load_ushort %0, %1, off" : "=v"(pr2) : "v"(logdt + p0 + pl) : "memory");

        const bf16* srcA[4]; const bf16* srcS[2]; const bf16* srcF[4];
#pragma unroll
        for (int i = 0; i < 4; ++i) {
            int row = (w * 4 + i) * 8 + (lane >> 3);
            int ch = (lane & 7) ^ ((row >> 1) & 7);
            srcA[i] = x + (size_t)(l0 + row) * D_ + ch * 8;
        }
#pragma unroll
        for (int i = 0; i < 2; ++i) {
            int n = (w * 2 + i) * 8 + (lane >> 3);
            int ch = (lane & 7) ^ ((n >> 1) & 7);
            const bf16* base = (n < 32) ? bre : bim;
            srcS[i] = base + (size_t)(p0 + (n & 31)) * D_ + ch * 8;
        }
#pragma unroll
        for (int i = 0; i < 4; ++i) {
            int ins = w * 4 + i;
            int blk = ins >> 3, r = (ins & 7) * 8 + (lane >> 3);
            int c = lane & 7;
            int fsw = ((r >> 1) & 1) | (((r >> 3) & 1) << 1);
            int col = f0 + blk * 64 + (((c >> 1) ^ fsw) << 4) + (c & 1) * 8;
            if (col > F_ - 8) col = F_ - 8;
            srcF[i] = w1 + (size_t)r * F_ + col;
        }
        const int krot = (rp + cg) & 7;
        auto issue = [&](int kt) {
            char* sb = smem + (kt & (K1_NST - 1)) * K1_STAGE;
            const int ko = ((kt + krot) & 7) * 64;
#pragma unroll
            for (int i = 0; i < 4; ++i) glds16(srcA[i] + ko, sb + (w * 4 + i) * 1024);
#pragma unroll
            for (int i = 0; i < 2; ++i) glds16(srcS[i] + ko, sb + K1_A + (w * 2 + i) * 1024);
#pragma unroll
            for (int i = 0; i < 4; ++i) glds16(srcF[i] + (size_t)ko * F_, sb + K1_A + K1_BS + (w * 4 + i) * 1024);
        };
        STAMP(1);
        issue(0); issue(1); issue(2); issue(3);
        STAMP(2);
        asm volatile("" ::: "memory");
        unsigned short w2v[2][8];
#pragma unroll
        for (int i = 0; i < 2; ++i) {
            int tl = w + 4 * i; if (tl > 6) tl = 6;
            int tile = blockIdx.x * 7 + tl;
            int nt = tile / KSF, ks = tile - nt * KSF;
            const unsigned short* src = reinterpret_cast<const unsigned short*>(w2) + (size_t)(ks * 32 + fq * 8) * D_ + nt * 16 + fr;
#pragma unroll
            for (int j = 0; j < 8; ++j) w2v[i][j] = src[(size_t)j * D_];
        }
        uint2 cvr, cvi;
        {
            int tile = blockIdx.x * 4 + w;
            int nt = tile >> 5, ks = tile & 31;
            size_t o = (size_t)(nt * 16 + fr) * P_ + ks * 16 + fq * 4;
            cvr = *reinterpret_cast<const uint2*>(cre + o);
            cvi = *reinterpret_cast<const uint2*>(cim + o);
        }
        const int grow = l0 + cg * 8 + w * 2;
        const u32x4 gwv = *reinterpret_cast<const u32x4*>(gw + lane * 8);
        const u32x4 gx0 = *reinterpret_cast<const u32x4*>(x + (size_t)grow * D_ + lane * 8);
        const u32x4 gx1 = *reinterpret_cast<const u32x4*>(x + (size_t)(grow + 1) * D_ + lane * 8);
        asm volatile("s_waitcnt vmcnt(61)" : "+v"(pr0), "+v"(pr1), "+v"(pr2) :: "memory");
        const float p_ar = bflo(pr0), p_ai = bflo(pr1);
        f32x2 lam, coef;
        {
            const float dt = expf(bflo(pr2));
            float e = expf(dt * p_ar), sn, cs;
            sincosf(dt * p_ai, &sn, &cs);
            lam.x = e * cs; lam.y = e * sn;
            float nr = lam.x - 1.f, ni = lam.y, den = p_ar * p_ar + p_ai * p_ai;
            coef.x = (nr * p_ar + ni * p_ai) / den; coef.y = (ni * p_ar - nr * p_ai) / den;
        }
        const f32x2 l2 = cmul(lam, lam), l4 = cmul(l2, l2), l8 = cmul(l4, l4), l16 = cmul(l8, l8);

        WAIT_VM(51);
        STAMP(3);
        BAR();
#define K1_LSTEP(t, NA)                                       \
        do {                                                  \
            WAIT_VM(NA);                                      \
            STAMP(4 + (t));                                   \
            BAR();                                            \
            if ((t) + 4 < 8) issue((t) + 4);                  \
        } while (0)
        K1_LSTEP(0, 41); K1_LSTEP(1, 41); K1_LSTEP(2, 41); K1_LSTEP(3, 20);
        K1_LSTEP(4, 20); K1_LSTEP(5, 10); K1_LSTEP(6, 0);
#undef K1_LSTEP
        LBAR();
        LBAR();
        __builtin_amdgcn_s_setprio(3);
        STAMP(11);
        {
            f32x2 hv[16]; f32x2 h = {0.f, 0.f};
            const char* up = ubuf + seg * USEG + pl * 8;
#pragma unroll
            for (int i = 0; i < 16; ++i) {
                f32x2 raw = *reinterpret_cast<const f32x2*>(up + i * US);
                f32x2 u = cmul(coef, raw);
                f32x2 t = cmul(lam, h);
                h.x = t.x + u.x; h.y = t.y + u.y; hv[i] = h;
            }
            f32x2 c = h;
            {
                const f32x2 l32 = cmul(l16, l16), l64 = cmul(l32, l32);
                f32x2 o;
                o.x = __shfl_up(c.x, 8); o.y = __shfl_up(c.y, 8);
                if (seg >= 1) { f32x2 t = cmul(l16, o); c.x += t.x; c.y += t.y; }
                o.x = __shfl_up(c.x, 16); o.y = __shfl_up(c.y, 16);
                if (seg >= 2) { f32x2 t = cmul(l32, o); c.x += t.x; c.y += t.y; }
                o.x = __shfl_up(c.x, 32); o.y = __shfl_up(c.y, 32);
                if (seg >= 4) { f32x2 t = cmul(l64, o); c.x += t.x; c.y += t.y; }
            }
            if (seg == 7) { u32x2 v = __builtin_bit_cast(u32x2, c); ST8(agg + ((size_t)rp * P_ + p0 + pl) * 2, v); }
            f32x2 cin; cin.x = __shfl_up(c.x, 8); cin.y = __shfl_up(c.y, 8);
            if (seg == 0) { cin.x = 0.f; cin.y = 0.f; }
            f32x2 t = cmul(cin, lam);
            char* hb = hbuf + w * 4096 + seg * 512 + ((lane >> 2) & 1) * 256 + (lane & 3) * 4;
#pragma unroll
            for (int i = 0; i < 16; ++i) {
                hv[i].x += t.x; hv[i].y += t.y; t = cmul(t, lam);
                *reinterpret_cast<unsigned*>(hb + i * 16) = pack2(hv[i].x, hv[i].y);
            }
            asm volatile("s_waitcnt lgkmcnt(0)" ::: "memory");
            const int ks = cg * 2 + (w >> 1);
#pragma unroll
            for (int i = 0; i < 4; ++i) {
                const int o = i * 1024 + lane * 16;
                const int sg = o >> 9;
                u32x4 v = *reinterpret_cast<const u32x4*>(hbuf + w * 4096 + o);
                char* dst = hloc_t + ((size_t)((rp * 2 + (sg >> 2)) * KSS + ks) * 4 + (sg & 3)) * 1024 + (w & 1) * 512 + (o & 511);
                ST16(dst, v);
            }
        }
        STAMP(12);
#pragma unroll
        for (int i = 0; i < 2; ++i) {
            int tl = w + 4 * i;
            if (tl < 7) {
                int tile = blockIdx.x * 7 + tl;
                int nt = tile / KSF, ks = tile - nt * KSF;
                u32x4 v;
                v.x = (unsigned)w2v[i][0] | ((unsigned)w2v[i][1] << 16); v.y = (unsigned)w2v[i][2] | ((unsigned)w2v[i][3] << 16);
                v.z = (unsigned)w2v[i][4] | ((unsigned)w2v[i][5] << 16); v.w = (unsigned)w2v[i][6] | ((unsigned)w2v[i][7] << 16);
                ST16(w2_t + ((size_t)((nt >> 2) * KSF + ks) * 4 + (nt & 3)) * 1024 + lane * 16, v);
            }
        }
        {
            int tile = blockIdx.x * 4 + w;
            int nt = tile >> 5, ks = tile & 31;
            unsigned ni0 = cvi.x ^ 0x80008000u, ni1 = cvi.y ^ 0x80008000u;
            u32x4 v;
            v.x = (cvr.x & 0xffffu) | (ni0 << 16); v.y = (cvr.x >> 16) | (ni0 & 0xffff0000u);
            v.z = (cvr.y & 0xffffu) | (ni1 << 16); v.w = (cvr.y >> 16) | (ni1 & 0xffff0000u);
            ST16(c_t + ((size_t)((nt >> 2) * KSS + ks) * 4 + (nt & 3)) * 1024 + lane * 16, v);
        }
        {
            float s0 = bflo(gx0.x) * bflo(gwv.x) + bfhi(gx0.x) * bfhi(gwv.x) + bflo(gx0.y) * bflo(gwv.y) + bfhi(gx0.y) * bfhi(gwv.y)
                     + bflo(gx0.z) * bflo(gwv.z) + bfhi(gx0.z) * bfhi(gwv.z) + bflo(gx0.w) * bflo(gwv.w) + bfhi(gx0.w) * bfhi(gwv.w);
            float s1 = bflo(gx1.x) * bflo(gwv.x) + bfhi(gx1.x) * bfhi(gwv.x) + bflo(gx1.y) * bflo(gwv.y) + bfhi(gx1.y) * bfhi(gwv.y)
                     + bflo(gx1.z) * bflo(gwv.z) + bfhi(gx1.z) * bfhi(gwv.z) + bflo(gx1.w) * bflo(gwv.w) + bfhi(gx1.w) * bfhi(gwv.w);
#pragma unroll
            for (int o = 32; o > 0; o >>= 1) { s0 += __shfl_xor(s0, o); s1 += __shfl_xor(s1, o); }
            if (lane == 0) {
                u32x2 v; v.x = __float_as_uint(1.f / (1.f + __expf(-s0))); v.y = __float_as_uint(1.f / (1.f + __expf(-s1)));
                ST8(gate + grow, v);
            }
        }
        if (rp == 0 && seg == 0) {
            f32x2 l32 = cmul(l16, l16), l64 = cmul(l32, l32), l128 = cmul(l64, l64);
            f32x4 v0 = {lam.x, lam.y, l16.x, l16.y}, v1 = {l64.x, l64.y, l128.x, l128.y};
            ST16(lamtab + (size_t)(p0 + pl) * 8, __builtin_bit_cast(u32x4, v0));
            ST16(lamtab + (size_t)(p0 + pl) * 8 + 4, __builtin_bit_cast(u32x4, v1));
        }
        STAMP_FLUSH(2);
        return;
    }

    uint2 b1v[7];
#pragma unroll
    for (int n = 0; n < 7; ++n) b1v[n] = *reinterpret_cast<const uint2*>(b1 + f0 + n * 16 + fq * 4);

    f32x4 acc[2][11];
#pragma unroll
    for (int m = 0; m < 2; ++m)
#pragma unroll
        for (int n = 0; n < 11; ++n) {
            if (n < 4) acc[m][n] = (f32x4){0.f, 0.f, 0.f, 0.f};
            else acc[m][n] = (f32x4){bflo(b1v[n - 4].x), bfhi(b1v[n - 4].x), bflo(b1v[n - 4].y), bfhi(b1v[n - 4].y)};
        }

    const unsigned smem_base = (unsigned)(size_t)smem;
    unsigned adA[2], adS[2], adF[2][4];
#pragma unroll
    for (int ks = 0; ks < 2; ++ks) {
        int ch = ks * 4 + fq;
        unsigned lo = fr * 128 + ((ch ^ ((fr >> 1) & 7)) << 4);
        adA[ks] = smem_base + w * 4096 + lo;
        adS[ks] = smem_base + K1_A + lo;
    }
    {
        const int q = (lane & 15) >> 2, pp = lane & 3;
        const int fsw = ((q >> 1) & 1) | ((fq & 1) << 1);
#pragma unroll
        for (int t = 0; t < 2; ++t)
#pragma unroll
            for (int pc = 0; pc < 4; ++pc)
                adF[t][pc] = smem_base + K1_A + K1_BS + (fq * 8 + 4 * t + q) * 128 + ((pc ^ fsw) << 5) + pp * 8;
    }
#define K1_ADV(delta)                                                                       \
    do {                                                                                    \
        _Pragma("unroll") for (int k_ = 0; k_ < 2; ++k_) { adA[k_] += (delta); adS[k_] += (delta); asm volatile("" : "+v"(adA[k_]), "+v"(adS[k_])); } \
        _Pragma("unroll") for (int t_ = 0; t_ < 2; ++t_) _Pragma("unroll") for (int c_ = 0; c_ < 4; ++c_) { adF[t_][c_] += (delta); asm volatile("" : "+v"(adF[t_][c_])); } \
    } while (0)
#define K1_RD(R, i, ks)                                                                     \
    do {                                                                                    \
        if ((i) == 0) DS_READ128(R.a[0], adA[ks], 0);                                       \
        if ((i) == 1) DS_READ128(R.a[1], adA[ks], 2048);                                    \
        if ((i) >= 2 && (i) < 6) DS_READ128(R.s[((i) - 2) & 3], adS[ks], (((i) - 2) & 3) * 2048); \
        if ((i) >= 6 && (i) < 20)                                                           \
            TR_READ(R.f[(((i) - 6) >> 1) % 7][(i) & 1], adF[(i) & 1][(((i) - 6) >> 1) & 3], (((((i) - 6) >> 1) % 7) >> 2) * 8192 + (ks) * 4096); \
    } while (0)
#define K1_WAITL(R)                                                                         \
    asm volatile("s_waitcnt lgkmcnt(0)"                                                     \
        : "+v"(R.a[0]), "+v"(R.a[1]), "+v"(R.s[0]), "+v"(R.s[1]), "+v"(R.s[2]), "+v"(R.s[3]), \
          "+v"(R.f[0][0]), "+v"(R.f[0][1]), "+v"(R.f[1][0]), "+v"(R.f[1][1]),               \
          "+v"(R.f[2][0]), "+v"(R.f[2][1]), "+v"(R.f[3][0]), "+v"(R.f[3][1]),               \
          "+v"(R.f[4][0]), "+v"(R.f[4][1]), "+v"(R.f[5][0]), "+v"(R.f[5][1]),               \
          "+v"(R.f[6][0]), "+v"(R.f[6][1]) :: "memory")
#define K1_MM(R, i)                                                                         \
    do {                                                                                    \
        if ((i) < 8) acc[(i) & 1][((i) >> 1) & 3] = __builtin_amdgcn_mfma_f32_16x16x32_bf16(R.s[((i) >> 1) & 3], R.a[(i) & 1], acc[(i) & 1][((i) >> 1) & 3], 0, 0, 0); \
        else {                                                                              \
            bf16x8 wb = __builtin_shufflevector(R.f[(((i) - 8) >> 1) % 7][0], R.f[(((i) - 8) >> 1) % 7][1], 0, 1, 2, 3, 4, 5, 6, 7); \
            acc[(i) & 1][4 + (((i) - 8) >> 1) % 7] = __builtin_amdgcn_mfma_f32_16x16x32_bf16(wb, R.a[(i) & 1], acc[(i) & 1][4 + (((i) - 8) >> 1) % 7], 0, 0, 0); \
        }                                                                                   \
    } while (0)
    {
        K1Frags R0, R1;
        BAR();
        asm volatile("" ::: "memory");
#pragma unroll
        for (int i = 0; i < 20; ++i) K1_RD(R0, i, 0);
        K1_WAITL(R0);
#define K1_STEP(t)                                            \
        do {                                                  \
            _Pragma("unroll") for (int i = 0; i < 22; ++i) { K1_MM(R0, i); K1_RD(R1, i, 1); SB0(); } \
            K1_WAITL(R1);                                     \
            if ((t) < 7) {                                    \
                K1_ADV((((t) & 3) == 3) ? -3 * K1_STAGE : K1_STAGE); \
                BAR();                                        \
                asm volatile("" ::: "memory");                \
                _Pragma("unroll") for (int i = 0; i < 22; ++i) { K1_MM(R1, i); K1_RD(R0, i, 0); SB0(); } \
                K1_WAITL(R0);                                 \
            } else {                                          \
                BAR();        \
                _Pragma("unroll") for (int i = 0; i < 22; ++i) K1_MM(R1, i); \
            }                                                 \
        } while (0)
        K1_STEP(0); STAMP(1); K1_STEP(1); K1_STEP(2); K1_STEP(3);
        STAMP(2); K1_STEP(4); K1_STEP(5); K1_STEP(6); K1_STEP(7); STAMP(3);
#undef K1_STEP
    }
#pragma unroll
    for (int m = 0; m < 2; ++m)
#pragma unroll
        for (int nt = 0; nt < 2; ++nt) {
            int pq = nt * 16 + fq * 4;
            char* up = ubuf + (w * 2 + m) * USEG + fr * US + pq * 8;
            f32x4 re = acc[m][nt], im = acc[m][nt + 2];
            f32x4 v0 = {re[0], im[0], re[1], im[1]}, v1 = {re[2], im[2], re[3], im[3]};
            *reinterpret_cast<f32x4*>(up) = v0;
            *reinterpret_cast<f32x4*>(up + 16) = v1;
        }
    LBAR();
    STAMP(4);
    {
        char* wimg = smem + 65536 + w * 8192;
        const int fbase = f0 & ~31;
        const int foff = (f0 & 31) ? 512 : 0;
#pragma unroll
        for (int n = 0; n < 7; ++n) {
            const int f = f0 + n * 16 + fq * 4;
            char* hp = wimg + ((f - fbase) >> 5) * 1024 + ((f >> 3) & 3) * 256 + fr * 16 + ((f >> 2) & 1) * 8 - foff;
#pragma unroll
            for (int m = 0; m < 2; ++m) {
                float o[4];
#pragma unroll
                for (int j = 0; j < 4; ++j) o[j] = gelu_tanh(acc[m][4 + n][j]);
                u32x2 pk; pk.x = pack2(o[0], o[1]); pk.y = pack2(o[2], o[3]);
                *reinterpret_cast<u32x2*>(hp + m * 3584) = pk;
            }
        }
        STAMP(5);
        const int lw = l0 + w * 32;
        char* hrow = H_t + ((size_t)(lw >> 6) * KSF * 4 + ((lw >> 4) & 3) + (size_t)(fbase >> 5) * 4) * 1024;
#pragma unroll
        for (int i = 0; i < 7; ++i) {
            int m, o;
            if (i < 6) { m = i / 3; o = (i % 3) * 1024 + lane * 16; }
            else { m = lane >> 5; o = 3072 + (lane & 31) * 16; }
            u32x4 v = *reinterpret_cast<const u32x4*>(wimg + m * 3584 + o);
            const int og = o + foff;
            ST16(hrow + m * 1024 + (og >> 10) * 4096 + (og & 1023), v);
        }
    }
    STAMP_FLUSH(0);
}

constexpr int K2_RS = 272;
constexpr int K2_RED = 0;
constexpr int K2_T1 = 0;
constexpr int K2_L16 = 65536;
constexpr int K2_LDS = 8 * 64 * K2_RS;
constexpr int K2_NFW = 4;
constexpr int K2_NFS = 14;
constexpr int K2_NSS = 8;
constexpr int K2_DEPTH = 4;

__global__ __launch_bounds__(512, 2) void k2_gemm_mix(
    const bf16* __restrict__ x, const bf16* __restrict__ dskip, const bf16* __restrict__ b2,
    const char* __restrict__ hloc_t, const float* __restrict__ agg,
    const char* __restrict__ H_t, const char* __restrict__ w2_t, const char* __restrict__ c_t,
    const float* __restrict__ gate, const float* __restrict__ lamtab, float* __restrict__ out)
{
    extern __shared__ __attribute__((aligned(16))) char smem[];
    const int tid = threadIdx.x, lane = tid & 63;
    const int wv = __builtin_amdgcn_readfirstlane(tid >> 6);
    const int xcd = blockIdx.x & 7, ii = blockIdx.x >> 3;
    const int rt = (xcd & 3) * 8 + (ii & 7);
    const int ct = (xcd >> 2) * 4 + (ii >> 3);
    const int l0 = rt * 64, d0 = ct * 64;
    const int fr = lane & 15, fq = lane >> 4;
    STAMP_DECL();
    const bool is_ffn = wv < K2_NFW;
    const int k0 = is_ffn ? wv : wv - K2_NFW;
    const int kst = is_ffn ? K2_NFW : 8 - K2_NFW;
    const int nst = is_ffn ? (KSF - k0 + kst - 1) / kst : (KSS - k0 + kst - 1) / kst;
    const char* pA = (is_ffn ? H_t + (size_t)(rt * KSF + k0) * 4096 : hloc_t + (size_t)(rt * KSS + k0) * 4096) + lane * 16;
    const char* pB = (is_ffn ? w2_t + (size_t)(ct * KSF + k0) * 4096 : c_t + (size_t)(ct * KSS + k0) * 4096) + lane * 16;
    const int sstride = kst * 4096;

    f32x4 acc[4][4];
#pragma unroll
    for (int n = 0; n < 4; ++n)
#pragma unroll
        for (int m = 0; m < 4; ++m) acc[n][m] = (f32x4){0.f, 0.f, 0.f, 0.f};

    float gl[4];
    const int chunk = rt >> 1, jbase = (rt & 1) * 64 + 1;
    const f32x4 lt0 = *reinterpret_cast<const f32x4*>(lamtab + (size_t)tid * 8);
    const f32x4 lt1 = *reinterpret_cast<const f32x4*>(lamtab + (size_t)tid * 8 + 4);
    f32x2 ag[NCH - 1];
#pragma unroll
    for (int c = 0; c < NCH - 1; ++c) ag[c] = *reinterpret_cast<const f32x2*>(agg + ((size_t)c * P_ + tid) * 2);
#pragma unroll
    for (int m = 0; m < 4; ++m) gl[m] = gate[l0 + m * 16 + fr];
    auto make_tables = [&]() {
        const f32x2 lam = {lt0[0], lt0[1]}, l16 = {lt0[2], lt0[3]};
        const f32x2 l64 = {lt1[0], lt1[1]}, l128 = {lt1[2], lt1[3]};
        f32x2 S = {0.f, 0.f};
#pragma unroll
        for (int c = 0; c < NCH - 1; ++c) {
            if (c < chunk) { f32x2 t = cmul(l128, S); S.x = t.x + ag[c].x; S.y = t.y + ag[c].y; }
        }
        f32x2 t = cmul(S, lam);
        if (jbase > 1) t = cmul(t, l64);
        f32x2* T1 = reinterpret_cast<f32x2*>(smem + K2_T1);
#pragma unroll
        for (int r = 0; r < 16; ++r) { T1[r * P_ + tid] = t; t = cmul(t, lam); }
        reinterpret_cast<f32x2*>(smem + K2_L16)[tid] = l16;
    };

    if (is_ffn) {
        constexpr int DP = 4;
        bf16x8 fa[DP][4], fb[DP][4];
        auto load_step = [&](int s) {
            const int slot = s % DP;
#pragma unroll
            for (int i = 0; i < 4; ++i) fa[slot][i] = *reinterpret_cast<const bf16x8*>(pA + (size_t)s * sstride + i * 1024);
#pragma unroll
            for (int i = 0; i < 4; ++i) fb[slot][i] = *reinterpret_cast<const bf16x8*>(pB + (size_t)s * sstride + i * 1024);
        };
        PIN();
#pragma unroll
        for (int s = 0; s < DP - 1; ++s) load_step(s);
        PIN();
        make_tables();
        STAMP(1);
#pragma unroll
        for (int s = 0; s < K2_NFS; ++s) {
            if (s + DP - 1 < K2_NFS) load_step(s + DP - 1);
            PIN();
            const int slot = s % DP;
#pragma unroll
            for (int n = 0; n < 4; ++n)
#pragma unroll
                for (int m = 0; m < 4; ++m) acc[n][m] = __builtin_amdgcn_mfma_f32_16x16x32_bf16(fb[slot][n], fa[slot][m], acc[n][m], 0, 0, 0);
            PIN();
            if (s == 0) { LBAR(); STAMP(2); }
        }
        STAMP(3);
#pragma unroll
        for (int m = 0; m < 4; ++m) gl[m] = 1.f - gl[m];
    } else {
        __builtin_amdgcn_s_setprio(2);
        constexpr int DP = 3;
        bf16x8 fa[DP][4], fb[DP][4];
        auto load_step = [&](int s) {
            const int slot = s % DP;
#pragma unroll
            for (int i = 0; i < 4; ++i) fa[slot][i] = *reinterpret_cast<const bf16x8*>(pA + (size_t)s * sstride + i * 1024);
#pragma unroll
            for (int i = 0; i < 4; ++i) fb[slot][i] = *reinterpret_cast<const bf16x8*>(pB + (size_t)s * sstride + i * 1024);
        };
        make_tables();
        STAMP(1);
        PIN();
#pragma unroll
        for (int s = 0; s < DP - 1; ++s) load_step(s);
        PIN();
        LBAR();
        STAMP(2);
        const char* T1b = smem + K2_T1 + (fr * P_ + fq * 4 + k0 * 16) * 8;
        const char* L16b = smem + K2_L16 + (fq * 4 + k0 * 16) * 8;
#pragma unroll
        for (int s = 0; s < K2_NSS; ++s) {
            if (s + DP - 1 < K2_NSS) load_step(s + DP - 1);
            PIN();
            const int slot = s % DP;
            const int pofs = s * (8 - K2_NFW) * 16 * 8;
            f32x4 t01 = *reinterpret_cast<const f32x4*>(T1b + pofs), t23 = *reinterpret_cast<const f32x4*>(T1b + pofs + 16);
            f32x4 m01 = *reinterpret_cast<const f32x4*>(L16b + pofs), m23 = *reinterpret_cast<const f32x4*>(L16b + pofs + 16);
            f32x2 t[4] = {{t01[0], t01[1]}, {t01[2], t01[3]}, {t23[0], t23[1]}, {t23[2], t23[3]}};
            const f32x2 mm[4] = {{m01[0], m01[1]}, {m01[2], m01[3]}, {m23[0], m23[1]}, {m23[2], m23[3]}};
#pragma unroll
            for (int m = 0; m < 4; ++m) {
                u32x4 o;
#pragma unroll
                for (int j = 0; j < 4; ++j) {
                    o[j] = pack2(t[j].x, t[j].y);
                    if (m < 3) t[j] = cmul(t[j], mm[j]);
                }
                const bf16x8 tf = __builtin_bit_cast(bf16x8, o);
#pragma unroll
                for (int n = 0; n < 4; ++n) {
                    acc[n][m] = __builtin_amdgcn_mfma_f32_16x16x32_bf16(fb[slot][n], fa[slot][m], acc[n][m], 0, 0, 0);
                    acc[n][m] = __builtin_amdgcn_mfma_f32_16x16x32_bf16(fb[slot][n], tf, acc[n][m], 0, 0, 0);
                }
            }
            PIN();
        }
        STAMP(3);
#pragma unroll
        for (int m = 0; m < 4; ++m) gl[m] = 2.f * gl[m];
    }
    LBAR();
    {
        char* red = smem + K2_RED + wv * 64 * K2_RS;
#pragma unroll
        for (int m = 0; m < 4; ++m)
#pragma unroll
            for (int n = 0; n < 4; ++n) {
                f32x4 v;
#pragma unroll
                for (int j = 0; j < 4; ++j) v[j] = gl[m] * acc[n][m][j];
                *reinterpret_cast<f32x4*>(red + (m * 16 + fr) * K2_RS + (n * 16 + fq * 4) * 4) = v;
            }
    }
    const int c4 = (tid & 15) * 4;
    const uint2 dv = *reinterpret_cast<const uint2*>(dskip + d0 + c4);
    const uint2 bv = *reinterpret_cast<const uint2*>(b2 + d0 + c4);
    uint2 xv[2]; float gr[2];
#pragma unroll
    for (int i = 0; i < 2; ++i) {
        const int row = (tid >> 4) + 32 * i;
        xv[i] = *reinterpret_cast<const uint2*>(x + (size_t)(l0 + row) * D_ + d0 + c4);
        gr[i] = gate[l0 + row];
    }
    LBAR();
    STAMP(4);
    {
        const float df[4] = {bflo(dv.x), bfhi(dv.x), bflo(dv.y), bfhi(dv.y)};
        const float bf[4] = {bflo(bv.x), bfhi(bv.x), bflo(bv.y), bfhi(bv.y)};
#pragma unroll
        for (int i = 0; i < 2; ++i) {
            const int row = (tid >> 4) + 32 * i;
            const float g = gr[i];
            const float xf[4] = {bflo(xv[i].x), bfhi(xv[i].x), bflo(xv[i].y), bfhi(xv[i].y)};
            f32x4 sacc = *reinterpret_cast<const f32x4*>(smem + K2_RED + row * K2_RS + c4 * 4);
#pragma unroll
            for (int ww = 1; ww < 8; ++ww) sacc += *reinterpret_cast<const f32x4*>(smem + K2_RED + (ww * 64 + row) * K2_RS + c4 * 4);
            f32x4 o;
#pragma unroll
            for (int j = 0; j < 4; ++j) o[j] = sacc[j] + g * xf[j] * df[j] + (1.f - g) * bf[j];
            *reinterpret_cast<f32x4*>(out + (size_t)(l0 + row) * D_ + d0 + c4) = o;
        }
    }
    if (wv == 0 || wv == 5) { STAMP_FLUSH_W(wv ? 3 : 1); }
}

extern "C" void kernel_launch(void* const* d_in, const int* in_sizes, int n_in,
                              void* d_out, int out_size, void* d_ws, size_t ws_size,
                              hipStream_t stream) {
    const bf16* x = (const bf16*)d_in[0];
    const bf16* lre = (const bf16*)d_in[1];
    const bf16* lim = (const bf16*)d_in[2];
    const bf16* bre = (const bf16*)d_in[3];
    const bf16* bim = (const bf16*)d_in[4];
    const bf16* cre = (const bf16*)d_in[5];
    const bf16* cim = (const bf16*)d_in[6];
    const bf16* logdt = (const bf16*)d_in[7];
    const bf16* dskip = (const bf16*)d_in[8];
    const bf16* w1 = (const bf16*)d_in[10];
    const bf16* b1 = (const bf16*)d_in[11];
    const bf16* w2 = (const bf16*)d_in[12];
    const bf16* b2 = (const bf16*)d_in[13];
    const bf16* gw = (const bf16*)d_in[14];

    char* ws = (char*)d_ws;
    char* hloc_t = ws;
    char* H_t = ws + (4u << 20);
    char* w2_t = ws + (12u << 20);
    char* c_t = ws + (14u << 20);
    float* agg = (float*)(ws + (16u << 20));
    float* gate = (float*)(ws + (16u << 20) + 65536);
    float* lamtab = (float*)(ws + (16u << 20) + 65536 + 8192);

    (void)hipFuncSetAttribute((const void*)k1_gemm_scan, hipFuncAttributeMaxDynamicSharedMemorySize, K1_LDS);
    (void)hipFuncSetAttribute((const void*)k2_gemm_mix, hipFuncAttributeMaxDynamicSharedMemorySize, K2_LDS);
    k1_gemm_scan<<<256, 512, K1_LDS, stream>>>(x, lre, lim, bre, bim, logdt, w1, b1, w2, cre, cim, gw, hloc_t, agg, H_t, w2_t, c_t, gate, lamtab);
    k2_gemm_mix<<<256, 512, K2_LDS, stream>>>(x, dskip, b2, hloc_t, agg, H_t, w2_t, c_t, gate, lamtab, (float*)d_out);
}
```

```cpp
#include <hip/hip_runtime.h>
#include <hip/hip_bf16.h>
#include <stdlib.h>

typedef __hip_bfloat16 bf16;
typedef __attribute__((ext_vector_type(8))) short bf16x8;
typedef __attribute__((ext_vector_type(4))) short bf16x4;
typedef __attribute__((ext_vector_type(4))) float f32x4;
typedef __attribute__((ext_vector_type(2))) float f32x2;
typedef __attribute__((ext_vector_type(4))) unsigned u32x4;
typedef __attribute__((ext_vector_type(2))) unsigned u32x2;

constexpr int L_ = 2048, D_ = 512, P_ = 512, F_ = 1792;
constexpr int T_ = 128;
constexpr int NCH = L_ / T_;
constexpr int KSF = F_ / 32;
constexpr int KSS = P_ / 16;

#define WAIT_VM(n) asm volatile("s_waitcnt vmcnt(" #n ")" ::: "memory")
#define BAR() __builtin_amdgcn_s_barrier()
#define LBAR() do { asm volatile("s_waitcnt lgkmcnt(0)" ::: "memory"); __builtin_amdgcn_s_barrier(); asm volatile("" ::: "memory"); } while (0)

#define STAMP_FLUSH_W(k) do {} while (0)
#define STAMP_DECL() do {} while (0)
#define STAMP(i) do {} while (0)
#define STAMP_FLUSH(k) do {} while (0)
#define TR_READ(dst, addr, off) asm volatile("ds_read_b64_tr_b16 %0, %1 offset:%2" : "=v"(dst) : "v"(addr), "i"(off) : "memory")
#define DS_READ128(dst, addr, off) asm volatile("ds_read_b128 %0, %1 offset:%2" : "=v"(dst) : "v"(addr), "i"(off) : "memory")
#define SB0() __builtin_amdgcn_sched_barrier(0)
#define PIN() do { asm volatile("" ::: "memory"); __builtin_amdgcn_sched_barrier(0); } while (0)
#define ST16(p, v) asm volatile("global_store_dwordx4 %0, %1, off sc0 sc1\n\ts_nop 1" :: "v"(p), "v"(v) : "memory")
#define ST8(p, v) asm volatile("global_store_dwordx2 %0, %1, off sc0 sc1\n\ts_nop 1" :: "v"(p), "v"(v) : "memory")

__device__ __forceinline__ float bflo(unsigned u) { return __uint_as_float(u << 16); }
__device__ __forceinline__ float bfhi(unsigned u) { return __uint_as_float(u & 0xffff0000u); }
typedef __attribute__((ext_vector_type(2))) __bf16 bf16x2_t;
__device__ __forceinline__ unsigned pack2(float lo, float hi) {
    f32x2 v = {lo, hi};
    return __builtin_bit_cast(unsigned, __builtin_convertvector(v, bf16x2_t));
}
__device__ __forceinline__ void glds16(const void* g, void* l) {
    __builtin_amdgcn_global_load_lds((const unsigned*)g, (unsigned*)l, 16, 0, 0);
}
__device__ __forceinline__ f32x2 cmul(f32x2 a, f32x2 b) {
    f32x2 r; r.x = a.x * b.x - a.y * b.y; r.y = a.x * b.y + a.y * b.x; return r;
}
__device__ __forceinline__ float gelu_tanh(float v) {
    const float k1 = -2.f * 0.7978845608028654f * 1.4426950408889634f;
    const float k2 = k1 * 0.044715f;
    float e = __builtin_amdgcn_exp2f(v * __builtin_fmaf(v * v, k2, k1));
    return v * __builtin_amdgcn_rcpf(1.f + e);
}

constexpr int K1_A = 128 * 128;
constexpr int K1_BS = 64 * 128;
constexpr int K1_BF = 2 * 64 * 128;
constexpr int K1_STAGE = K1_A + K1_BS + K1_BF;
constexpr int K1_NST = 4;
constexpr int K1_LDS = K1_STAGE * K1_NST;

struct K1Frags { bf16x8 a[2]; bf16x8 s[4]; bf16x4 f[7][2]; };

__global__ __launch_bounds__(512, 2) void k1_gemm_scan(
    const bf16* __restrict__ x, const bf16* __restrict__ lre, const bf16* __restrict__ lim,
    const bf16* __restrict__ bre, const bf16* __restrict__ bim, const bf16* __restrict__ logdt,
    const bf16* __restrict__ w1, const bf16* __restrict__ b1,
    const bf16* __restrict__ w2, const bf16* __restrict__ cre, const bf16* __restrict__ cim,
    const bf16* __restrict__ gw,
    char* __restrict__ hloc_t, float* __restrict__ agg, char* __restrict__ H_t,
    char* __restrict__ w2_t, char* __restrict__ c_t, float* __restrict__ gate, float* __restrict__ lamtab)
{
    extern __shared__ __attribute__((aligned(16))) char smem[];
    const int tid = threadIdx.x, lane = tid & 63;
    const int wv = __builtin_amdgcn_readfirstlane(tid >> 6);
    const int w = wv & 3;
    const int xcd = blockIdx.x & 7, ii = blockIdx.x >> 3;
    const int rp = xcd * 2 + (ii & 1);
    const int cg = ii >> 1;
    const int l0 = rp * T_, p0 = cg * 32, f0 = cg * 112;
    const int fr = lane & 15, fq = lane >> 4;
    STAMP_DECL();
    constexpr int US = 272, USEG = 16 * US + 64;
    char* ubuf = smem;
    char* pbuf = smem + 36864;
    char* hbuf = smem + 40960;

    if (wv >= 4) {
        const int pl = w * 8 + (lane & 7), seg = lane >> 3;
        const bf16* srcA[4]; const bf16* srcS[2]; const bf16* srcF[4];
#pragma unroll
        for (int i = 0; i < 4; ++i) {
            int row = (w * 4 + i) * 8 + (lane >> 3);
            int ch = (lane & 7) ^ ((row >> 1) & 7);
            srcA[i] = x + (size_t)(l0 + row) * D_ + ch * 8;
        }
#pragma unroll
        for (int i = 0; i < 2; ++i) {
            int n = (w * 2 + i) * 8 + (lane >> 3);
            int ch = (lane & 7) ^ ((n >> 1) & 7);
            const bf16* base = (n < 32) ? bre : bim;
            srcS[i] = base + (size_t)(p0 + (n & 31)) * D_ + ch * 8;
        }
#pragma unroll
        for (int i = 0; i < 4; ++i) {
            int ins = w * 4 + i;
            int blk = ins >> 3, r = (ins & 7) * 8 + (lane >> 3);
            int c = lane & 7;
            int fsw = ((r >> 1) & 1) | (((r >> 3) & 1) << 1);
            int col = f0 + blk * 64 + (((c >> 1) ^ fsw) << 4) + (c & 1) * 8;
            if (col > F_ - 8) col = F_ - 8;
            srcF[i] = w1 + (size_t)r * F_ + col;
        }
        const int krot = (rp + cg) & 7;
        auto issue = [&](int kt) {
            char* sb = smem + (kt & (K1_NST - 1)) * K1_STAGE;
            const int ko = ((kt + krot) & 7) * 64;
#pragma unroll
            for (int i = 0; i < 4; ++i) glds16(srcA[i] + ko, sb + (w * 4 + i) * 1024);
#pragma unroll
            for (int i = 0; i < 2; ++i) glds16(srcS[i] + ko, sb + K1_A + (w * 2 + i) * 1024);
#pragma unroll
            for (int i = 0; i < 4; ++i) glds16(srcF[i] + (size_t)ko * F_, sb + K1_A + K1_BS + (w * 4 + i) * 1024);
        };
        STAMP(1);
        issue(0); issue(1);
        WAIT_VM(10);
        STAMP(3);
        BAR();
        issue(2); issue(3);
        STAMP(2);
        asm volatile("" ::: "memory");
#define K1_LSTEP(t, NA)                                       \
        do {                                                  \
            WAIT_VM(NA);                                      \
            STAMP(4 + (t));                                   \
            BAR();                                            \
            if ((t) + 4 < 8) issue((t) + 4);                  \
        } while (0)
        K1_LSTEP(0, 20); K1_LSTEP(1, 20); K1_LSTEP(2, 20); K1_LSTEP(3, 20);
        unsigned w2v[2][8];
#pragma unroll
        for (int i = 0; i < 2; ++i) {
            int tl = w + 4 * i; if (tl > 6) tl = 6;
            int tile = blockIdx.x * 7 + tl;
            int nt = tile / KSF, ks = tile - nt * KSF;
            const unsigned short* src = reinterpret_cast<const unsigned short*>(w2) + (size_t)(ks * 32 + fq * 8) * D_ + nt * 16 + fr;
#pragma unroll
            for (int j = 0; j < 8; ++j) asm volatile("global_load_ushort %0, %1, off offset:%2" : "=v"(w2v[i][j]) : "v"(src + (j >> 2) * 4 * D_), "i"((j & 3) * D_ * 2) : "memory");
        }
        u32x2 cvr, cvi;
        {
            int tile = blockIdx.x * 4 + w;
            int nt = tile >> 5, ks = tile & 31;
            size_t o = (size_t)(nt * 16 + fr) * P_ + ks * 16 + fq * 4;
            asm volatile("global_load_dwordx2 %0, %1, off" : "=v"(cvr) : "v"(cre + o) : "memory");
            asm volatile("global_load_dwordx2 %0, %1, off" : "=v"(cvi) : "v"(cim + o) : "memory");
        }
        const int grow = l0 + cg * 8 + w * 2;
        u32x4 gwv, gx0, gx1;
        asm volatile("global_load_dwordx4 %0, %1, off" : "=v"(gwv) : "v"(gw + lane * 8) : "memory");
        asm volatile("global_load_dwordx4 %0, %1, off" : "=v"(gx0) : "v"(x + (size_t)grow * D_ + lane * 8) : "memory");
        asm volatile("global_load_dwordx4 %0, %1, off offset:1024" : "=v"(gx1) : "v"(x + (size_t)grow * D_ + lane * 8) : "memory");
        K1_LSTEP(4, 41); K1_LSTEP(5, 31); K1_LSTEP(6, 21);
#undef K1_LSTEP
        LBAR();
        WAIT_VM(0);
        LBAR();
        __builtin_amdgcn_s_setprio(3);
        STAMP(11);
        const f32x4 pc = *reinterpret_cast<const f32x4*>(pbuf + pl * 16);
        const f32x2 lam = {pc[0], pc[1]}, coef = {pc[2], pc[3]};
        const f32x2 l2 = cmul(lam, lam), l4 = cmul(l2, l2), l8 = cmul(l4, l4), l16 = cmul(l8, l8);
        {
            f32x2 hv[16]; f32x2 h = {0.f, 0.f};
            const char* up = ubuf + seg * USEG + pl * 8;
#pragma unroll
            for (int i = 0; i < 16; ++i) {
                f32x2 raw = *reinterpret_cast<const f32x2*>(up + i * US);
                f32x2 u = cmul(coef, raw);
                f32x2 t = cmul(lam, h);
                h.x = t.x + u.x; h.y = t.y + u.y; hv[i] = h;
            }
            f32x2 c = h;
            {
                const f32x2 l32 = cmul(l16, l16), l64 = cmul(l32, l32);
                f32x2 o;
                o.x = __shfl_up(c.x, 8); o.y = __shfl_up(c.y, 8);
                if (seg >= 1) { f32x2 t = cmul(l16, o); c.x += t.x; c.y += t.y; }
                o.x = __shfl_up(c.x, 16); o.y = __shfl_up(c.y, 16);
                if (seg >= 2) { f32x2 t = cmul(l32, o); c.x += t.x; c.y += t.y; }
                o.x = __shfl_up(c.x, 32); o.y = __shfl_up(c.y, 32);
                if (seg >= 4) { f32x2 t = cmul(l64, o); c.x += t.x; c.y += t.y; }
            }
            if (seg == 7) { u32x2 v = __builtin_bit_cast(u32x2, c); ST8(agg + ((size_t)rp * P_ + p0 + pl) * 2, v); }
            f32x2 cin; cin.x = __shfl_up(c.x, 8); cin.y = __shfl_up(c.y, 8);
            if (seg == 0) { cin.x = 0.f; cin.y = 0.f; }
            f32x2 t = cmul(cin, lam);
            char* hb = hbuf + w * 4096 + seg * 512 + ((lane >> 2) & 1) * 256 + (lane & 3) * 4;
#pragma unroll
            for (int i = 0; i < 16; ++i) {
                hv[i].x += t.x; hv[i].y += t.y; t = cmul(t, lam);
                *reinterpret_cast<unsigned*>(hb + i * 16) = pack2(hv[i].x, hv[i].y);
            }
            asm volatile("s_waitcnt lgkmcnt(0)" ::: "memory");
            const int ks = cg * 2 + (w >> 1);
#pragma unroll
            for (int i = 0; i < 4; ++i) {
                const int o = i * 1024 + lane * 16;
                const int sg = o >> 9;
                u32x4 v = *reinterpret_cast<const u32x4*>(hbuf + w * 4096 + o);
                char* dst = hloc_t + ((size_t)((rp * 2 + (sg >> 2)) * KSS + ks) * 4 + (sg & 3)) * 1024 + (w & 1) * 512 + (o & 511);
                ST16(dst, v);
            }
        }
        STAMP(12);
        if (rp == 0 && seg == 0) {
            f32x2 l32 = cmul(l16, l16), l64 = cmul(l32, l32), l128 = cmul(l64, l64);
            f32x4 v0 = {lam.x, lam.y, l16.x, l16.y}, v1 = {l64.x, l64.y, l128.x, l128.y};
            ST16(lamtab + (size_t)(p0 + pl) * 8, __builtin_bit_cast(u32x4, v0));
            ST16(lamtab + (size_t)(p0 + pl) * 8 + 4, __builtin_bit_cast(u32x4, v1));
        }
        asm volatile("" : "+v"(w2v[0][0]), "+v"(w2v[0][1]), "+v"(w2v[0][2]), "+v"(w2v[0][3]), "+v"(w2v[0][4]), "+v"(w2v[0][5]), "+v"(w2v[0][6]), "+v"(w2v[0][7]),
                          "+v"(w2v[1][0]), "+v"(w2v[1][1]), "+v"(w2v[1][2]), "+v"(w2v[1][3]), "+v"(w2v[1][4]), "+v"(w2v[1][5]), "+v"(w2v[1][6]), "+v"(w2v[1][7]),
                          "+v"(cvr), "+v"(cvi), "+v"(gwv), "+v"(gx0), "+v"(gx1) :: "memory");
#pragma unroll
        for (int i = 0; i < 2; ++i) {
            int tl = w + 4 * i;
            if (tl < 7) {
                int tile = blockIdx.x * 7 + tl;
                int nt = tile / KSF, ks = tile - nt * KSF;
                u32x4 v;
                v.x = w2v[i][0] | (w2v[i][1] << 16); v.y = w2v[i][2] | (w2v[i][3] << 16);
                v.z = w2v[i][4] | (w2v[i][5] << 16); v.w = w2v[i][6] | (w2v[i][7] << 16);
                ST16(w2_t + ((size_t)((nt >> 2) * KSF + ks) * 4 + (nt & 3)) * 1024 + lane * 16, v);
            }
        }
        {
            int tile = blockIdx.x * 4 + w;
            int nt = tile >> 5, ks = tile & 31;
            unsigned ni0 = cvi.x ^ 0x80008000u, ni1 = cvi.y ^ 0x80008000u;
            u32x4 v;
            v.x = (cvr.x & 0xffffu) | (ni0 << 16); v.y = (cvr.x >> 16) | (ni0 & 0xffff0000u);
            v.z = (cvr.y & 0xffffu) | (ni1 << 16); v.w = (cvr.y >> 16) | (ni1 & 0xffff0000u);
            ST16(c_t + ((size_t)((nt >> 2) * KSS + ks) * 4 + (nt & 3)) * 1024 + lane * 16, v);
        }
        {
            float s0 = bflo(gx0.x) * bflo(gwv.x) + bfhi(gx0.x) * bfhi(gwv.x) + bflo(gx0.y) * bflo(gwv.y) + bfhi(gx0.y) * bfhi(gwv.y)
                     + bflo(gx0.z) * bflo(gwv.z) + bfhi(gx0.z) * bfhi(gwv.z) + bflo(gx0.w) * bflo(gwv.w) + bfhi(gx0.w) * bfhi(gwv.w);
            float s1 = bflo(gx1.x) * bflo(gwv.x) + bfhi(gx1.x) * bfhi(gwv.x) + bflo(gx1.y) * bflo(gwv.y) + bfhi(gx1.y) * bfhi(gwv.y)
                     + bflo(gx1.z) * bflo(gwv.z) + bfhi(gx1.z) * bfhi(gwv.z) + bflo(gx1.w) * bflo(gwv.w) + bfhi(gx1.w) * bfhi(gwv.w);
#pragma unroll
            for (int o = 32; o > 0; o >>= 1) { s0 += __shfl_xor(s0, o); s1 += __shfl_xor(s1, o); }
            if (lane == 0) {
                u32x2 v; v.x = __float_as_uint(1.f / (1.f + __expf(-s0))); v.y = __float_as_uint(1.f / (1.f + __expf(-s1)));
                ST8(gate + grow, v);
            }
        }
        STAMP_FLUSH(2);
        return;
    }

    f32x4 pconst;
    {
        const int p = p0 + w * 8 + (lane & 7);
        const float p_ar = bflo(*reinterpret_cast<const unsigned short*>(lre + p));
        const float p_ai = bflo(*reinterpret_cast<const unsigned short*>(lim + p));
        const float dt = expf(bflo(*reinterpret_cast<const unsigned short*>(logdt + p)));
        float ee = expf(dt * p_ar), sn, cs;
        sincosf(dt * p_ai, &sn, &cs);
        const float lx = ee * cs, ly = ee * sn;
        const float nr = lx - 1.f, ni = ly, den = p_ar * p_ar + p_ai * p_ai;
        pconst = (f32x4){lx, ly, (nr * p_ar + ni * p_ai) / den, (ni * p_ar - nr * p_ai) / den};
    }
    uint2 b1v[7];
#pragma unroll
    for (int n = 0; n < 7; ++n) b1v[n] = *reinterpret_cast<const uint2*>(b1 + f0 + n * 16 + fq * 4);

    f32x4 acc[2][11];
#pragma unroll
    for (int m = 0; m < 2; ++m)
#pragma unroll
        for (int n = 0; n < 11; ++n) {
            if (n < 4) acc[m][n] = (f32x4){0.f, 0.f, 0.f, 0.f};
            else acc[m][n] = (f32x4){bflo(b1v[n - 4].x), bfhi(b1v[n - 4].x), bflo(b1v[n - 4].y), bfhi(b1v[n - 4].y)};
        }

    const unsigned smem_base = (unsigned)(size_t)smem;
    unsigned adA[2], adS[2], adF[2][4];
#pragma unroll
    for (int ks = 0; ks < 2; ++ks) {
        int ch = ks * 4 + fq;
        unsigned lo = fr * 128 + ((ch ^ ((fr >> 1) & 7)) << 4);
        adA[ks] = smem_base + w * 4096 + lo;
        adS[ks] = smem_base + K1_A + lo;
    }
    {
        const int q = (lane & 15) >> 2, pp = lane & 3;
        const int fsw = ((q >> 1) & 1) | ((fq & 1) << 1);
#pragma unroll
        for (int t = 0; t < 2; ++t)
#pragma unroll
            for (int pc = 0; pc < 4; ++pc)
                adF[t][pc] = smem_base + K1_A + K1_BS + (fq * 8 + 4 * t + q) * 128 + ((pc ^ fsw) << 5) + pp * 8;
    }
#define K1_ADV(delta)                                                                       \
    do {                                                                                    \
        _Pragma("unroll") for (int k_ = 0; k_ < 2; ++k_) { adA[k_] += (delta); adS[k_] += (delta); asm volatile("" : "+v"(adA[k_]), "+v"(adS[k_])); } \
        _Pragma("unroll") for (int t_ = 0; t_ < 2; ++t_) _Pragma("unroll") for (int c_ = 0; c_ < 4; ++c_) { adF[t_][c_] += (delta); asm volatile("" : "+v"(adF[t_][c_])); } \
    } while (0)
#define K1_RD(R, i, ks)                                                                     \
    do {                                                                                    \
        if ((i) == 0) DS_READ128(R.a[0], adA[ks], 0);                                       \
        if ((i) == 1) DS_READ128(R.a[1], adA[ks], 2048);                                    \
        if ((i) >= 2 && (i) < 6) DS_READ128(R.s[((i) - 2) & 3], adS[ks], (((i) - 2) & 3) * 2048); \
        if ((i) >= 6 && (i) < 20)                                                           \
            TR_READ(R.f[(((i) - 6) >> 1) % 7][(i) & 1], adF[(i) & 1][(((i) - 6) >> 1) & 3], (((((i) - 6) >> 1) % 7) >> 2) * 8192 + (ks) * 4096); \
    } while (0)
#define K1_WAITL(R)                                                                         \
    asm volatile("s_waitcnt lgkmcnt(0)"                                                     \
        : "+v"(R.a[0]), "+v"(R.a[1]), "+v"(R.s[0]), "+v"(R.s[1]), "+v"(R.s[2]), "+v"(R.s[3]), \
          "+v"(R.f[0][0]), "+v"(R.f[0][1]), "+v"(R.f[1][0]), "+v"(R.f[1][1]),               \
          "+v"(R.f[2][0]), "+v"(R.f[2][1]), "+v"(R.f[3][0]), "+v"(R.f[3][1]),               \
          "+v"(R.f[4][0]), "+v"(R.f[4][1]), "+v"(R.f[5][0]), "+v"(R.f[5][1]),               \
          "+v"(R.f[6][0]), "+v"(R.f[6][1]) :: "memory")
#define K1_MM(R, i)                                                                         \
    do {                                                                                    \
        if ((i) < 8) acc[(i) & 1][((i) >> 1) & 3] = __builtin_amdgcn_mfma_f32_16x16x32_bf16(R.s[((i) >> 1) & 3], R.a[(i) & 1], acc[(i) & 1][((i) >> 1) & 3], 0, 0, 0); \
        else {                                                                              \
            bf16x8 wb = __builtin_shufflevector(R.f[(((i) - 8) >> 1) % 7][0], R.f[(((i) - 8) >> 1) % 7][1], 0, 1, 2, 3, 4, 5, 6, 7); \
            acc[(i) & 1][4 + (((i) - 8) >> 1) % 7] = __builtin_amdgcn_mfma_f32_16x16x32_bf16(wb, R.a[(i) & 1], acc[(i) & 1][4 + (((i) - 8) >> 1) % 7], 0, 0, 0); \
        }                                                                                   \
    } while (0)
    {
        K1Frags R0, R1;
        BAR();
        asm volatile("" ::: "memory");
#pragma unroll
        for (int i = 0; i < 20; ++i) K1_RD(R0, i, 0);
        K1_WAITL(R0);
#define K1_STEP(t)                                            \
        do {                                                  \
            _Pragma("unroll") for (int i = 0; i < 22; ++i) { K1_MM(R0, i); K1_RD(R1, i, 1); SB0(); } \
            K1_WAITL(R1);                                     \
            if ((t) < 7) {                                    \
                K1_ADV((((t) & 3) == 3) ? -3 * K1_STAGE : K1_STAGE); \
                BAR();                                        \
                asm volatile("" ::: "memory");                \
                _Pragma("unroll") for (int i = 0; i < 22; ++i) { K1_MM(R1, i); K1_RD(R0, i, 0); SB0(); } \
                K1_WAITL(R0);                                 \
            } else {                                          \
                BAR();        \
                _Pragma("unroll") for (int i = 0; i < 22; ++i) K1_MM(R1, i); \
            }                                                 \
        } while (0)
        K1_STEP(0); STAMP(1); K1_STEP(1); K1_STEP(2); K1_STEP(3);
        STAMP(2); K1_STEP(4); K1_STEP(5); K1_STEP(6); K1_STEP(7); STAMP(3);
#undef K1_STEP
    }
#pragma unroll
    for (int m = 0; m < 2; ++m)
#pragma unroll
        for (int nt = 0; nt < 2; ++nt) {
            int pq = nt * 16 + fq * 4;
            char* up = ubuf + (w * 2 + m) * USEG + fr * US + pq * 8;
            f32x4 re = acc[m][nt], im = acc[m][nt + 2];
            f32x4 v0 = {re[0], im[0], re[1], im[1]}, v1 = {re[2], im[2], re[3], im[3]};
            *reinterpret_cast<f32x4*>(up) = v0;
            *reinterpret_cast<f32x4*>(up + 16) = v1;
        }
    if (lane < 8) *reinterpret_cast<f32x4*>(pbuf + (w * 8 + lane) * 16) = pconst;
    LBAR();
    STAMP(4);
    {
        char* wimg = smem + 65536 + w * 8192;
        const int fbase = f0 & ~31;
        const int foff = (f0 & 31) ? 512 : 0;
#pragma unroll
        for (int n = 0; n < 7; ++n) {
            const int f = f0 + n * 16 + fq * 4;
            char* hp = wimg + ((f - fbase) >> 5) * 1024 + ((f >> 3) & 3) * 256 + fr * 16 + ((f >> 2) & 1) * 8 - foff;
#pragma unroll
            for (int m = 0; m < 2; ++m) {
                float o[4];
#pragma unroll
                for (int j = 0; j < 4; ++j) o[j] = gelu_tanh(acc[m][4 + n][j]);
                u32x2 pk; pk.x = pack2(o[0], o[1]); pk.y = pack2(o[2], o[3]);
                *reinterpret_cast<u32x2*>(hp + m * 3584) = pk;
            }
        }
        STAMP(5);
        const int lw = l0 + w * 32;
        char* hrow = H_t + ((size_t)(lw >> 6) * KSF * 4 + ((lw >> 4) & 3) + (size_t)(fbase >> 5) * 4) * 1024;
#pragma unroll
        for (int i = 0; i < 7; ++i) {
            int m, o;
            if (i < 6) { m = i / 3; o = (i % 3) * 1024 + lane * 16; }
            else { m = lane >> 5; o = 3072 + (lane & 31) * 16; }
            u32x4 v = *reinterpret_cast<const u32x4*>(wimg + m * 3584 + o);
            const int og = o + foff;
            ST16(hrow + m * 1024 + (og >> 10) * 4096 + (og & 1023), v);
        }
    }
    STAMP_FLUSH(0);
}

constexpr int K2_RS = 272;
constexpr int K2_RED = 0;
constexpr int K2_T1 = 0;
constexpr int K2_L16 = 69632;
constexpr int K2_LDS = 8 * 64 * K2_RS;
constexpr int K2_NFW = 4;
constexpr int K2_NFS = 14;
constexpr int K2_NSS = 8;
constexpr int K2_DEPTH = 4;

__global__ __launch_bounds__(512, 2) void k2_gemm_mix(
    const bf16* __restrict__ x, const bf16* __restrict__ dskip, const bf16* __restrict__ b2,
    const char* __restrict__ hloc_t, const float* __restrict__ agg,
    const char* __restrict__ H_t, const char* __restrict__ w2_t, const char* __restrict__ c_t,
    const float* __restrict__ gate, const float* __restrict__ lamtab, float* __restrict__ out)
{
    extern __shared__ __attribute__((aligned(16))) char smem[];
    const int tid = threadIdx.x, lane = tid & 63;
    const int wv = __builtin_amdgcn_readfirstlane(tid >> 6);
    const int xcd = blockIdx.x & 7, ii = blockIdx.x >> 3;
    const int rt = xcd * 4 + (ii & 3);
    const int ct = ii >> 2;
    const int l0 = rt * 64, d0 = ct * 64;
    const int fr = lane & 15, fq = lane >> 4;
    STAMP_DECL();
    const bool is_ffn = wv < K2_NFW;
    const int k0 = is_ffn ? wv : wv - K2_NFW;
    const int kst = is_ffn ? K2_NFW : 8 - K2_NFW;
    const int nst = is_ffn ? (KSF - k0 + kst - 1) / kst : (KSS - k0 + kst - 1) / kst;
    const char* pA = (is_ffn ? H_t + (size_t)(rt * KSF + k0) * 4096 : hloc_t + (size_t)(rt * KSS + k0) * 4096) + lane * 16;
    const char* pB = (is_ffn ? w2_t + (size_t)(ct * KSF + k0) * 4096 : c_t + (size_t)(ct * KSS + k0) * 4096) + lane * 16;
    const int sstride = kst * 4096;

    f32x4 acc[4][4];
#pragma unroll
    for (int n = 0; n < 4; ++n)
#pragma unroll
        for (int m = 0; m < 4; ++m) acc[n][m] = (f32x4){0.f, 0.f, 0.f, 0.f};

    float gl[4];
#pragma unroll
    for (int m = 0; m < 4; ++m) gl[m] = gate[l0 + m * 16 + fr];

    if (is_ffn) {
        constexpr int DP = 4;
        bf16x8 fa[DP][4], fb[DP][4];
        auto load_step = [&](int s) {
            const int slot = s % DP;
#pragma unroll
            for (int i = 0; i < 4; ++i) fa[slot][i] = *reinterpret_cast<const bf16x8*>(pA + (size_t)s * sstride + i * 1024);
#pragma unroll
            for (int i = 0; i < 4; ++i) fb[slot][i] = *reinterpret_cast<const bf16x8*>(pB + (size_t)s * sstride + i * 1024);
        };
        PIN();
#pragma unroll
        for (int s = 0; s < DP - 1; ++s) load_step(s);
        PIN();
#pragma unroll
        for (int s = 0; s < K2_NFS; ++s) {
            if (s + DP - 1 < K2_NFS) load_step(s + DP - 1);
            PIN();
            const int slot = s % DP;
#pragma unroll
            for (int n = 0; n < 4; ++n)
#pragma unroll
                for (int m = 0; m < 4; ++m) acc[n][m] = __builtin_amdgcn_mfma_f32_16x16x32_bf16(fb[slot][n], fa[slot][m], acc[n][m], 0, 0, 0);
            PIN();
        }
        STAMP(3);
#pragma unroll
        for (int m = 0; m < 4; ++m) gl[m] = 1.f - gl[m];
    } else {
        __builtin_amdgcn_s_setprio(2);
        constexpr int DP = 3;
        bf16x8 fa[DP][4], fb[DP][4];
        auto load_step = [&](int s) {
            const int slot = s % DP;
#pragma unroll
            for (int i = 0; i < 4; ++i) fa[slot][i] = *reinterpret_cast<const bf16x8*>(pA + (size_t)s * sstride + i * 1024);
#pragma unroll
            for (int i = 0; i < 4; ++i) fb[slot][i] = *reinterpret_cast<const bf16x8*>(pB + (size_t)s * sstride + i * 1024);
        };
        char* T1w = smem + K2_T1 + k0 * 17408;
        char* L16w = smem + K2_L16 + k0 * 1024;
        {
            const int chunk = rt >> 1, jbase = (rt & 1) * 64 + 1;
            f32x4 lt0[2], lt1[2]; f32x2 ag[2][NCH - 1];
#pragma unroll
            for (int h = 0; h < 2; ++h) {
                const int q = lane + h * 64;
                const int p = ((q >> 4) * 4 + k0) * 16 + (q & 15);
                lt0[h] = *reinterpret_cast<const f32x4*>(lamtab + (size_t)p * 8);
                lt1[h] = *reinterpret_cast<const f32x4*>(lamtab + (size_t)p * 8 + 4);
#pragma unroll
                for (int c = 0; c < NCH - 1; ++c) ag[h][c] = *reinterpret_cast<const f32x2*>(agg + ((size_t)c * P_ + p) * 2);
            }
            PIN();
            load_step(0);
            PIN();
#pragma unroll
            for (int h = 0; h < 2; ++h) {
                const int q = lane + h * 64;
                const f32x2 lam = {lt0[h][0], lt0[h][1]}, l16 = {lt0[h][2], lt0[h][3]};
                const f32x2 l64 = {lt1[h][0], lt1[h][1]}, l128 = {lt1[h][2], lt1[h][3]};
                f32x2 S = {0.f, 0.f};
#pragma unroll
                for (int c = 0; c < NCH - 1; ++c) {
                    if (c < chunk) { f32x2 t = cmul(l128, S); S.x = t.x + ag[h][c].x; S.y = t.y + ag[h][c].y; }
                }
                f32x2 t = cmul(S, lam);
                if (jbase > 1) t = cmul(t, l64);
#pragma unroll
                for (int r = 0; r < 16; ++r) { *reinterpret_cast<f32x2*>(T1w + (r * 128 + q) * 8 + r * 16) = t; t = cmul(t, lam); }
                *reinterpret_cast<f32x2*>(L16w + q * 8) = l16;
            }
        }
        STAMP(1);
        PIN();
#pragma unroll
        for (int s = 1; s < DP - 1; ++s) load_step(s);
        PIN();
        asm volatile("s_waitcnt lgkmcnt(0)" ::: "memory");
        STAMP(2);
        const char* T1b = T1w + (fr * 128 + fq * 4) * 8 + fr * 16;
        const char* L16b = L16w + (fq * 4) * 8;
#pragma unroll
        for (int s = 0; s < K2_NSS; ++s) {
            if (s + DP - 1 < K2_NSS) load_step(s + DP - 1);
            PIN();
            const int slot = s % DP;
            const int pofs = s * 16 * 8;
            f32x4 t01 = *reinterpret_cast<const f32x4*>(T1b + pofs), t23 = *reinterpret_cast<const f32x4*>(T1b + pofs + 16);
            f32x4 m01 = *reinterpret_cast<const f32x4*>(L16b + pofs), m23 = *reinterpret_cast<const f32x4*>(L16b + pofs + 16);
            f32x2 t[4] = {{t01[0], t01[1]}, {t01[2], t01[3]}, {t23[0], t23[1]}, {t23[2], t23[3]}};
            const f32x2 mm[4] = {{m01[0], m01[1]}, {m01[2], m01[3]}, {m23[0], m23[1]}, {m23[2], m23[3]}};
#pragma unroll
            for (int m = 0; m < 4; ++m) {
                u32x4 o;
#pragma unroll
                for (int j = 0; j < 4; ++j) {
                    o[j] = pack2(t[j].x, t[j].y);
                    if (m < 3) t[j] = cmul(t[j], mm[j]);
                }
                const bf16x8 tf = __builtin_bit_cast(bf16x8, o);
#pragma unroll
                for (int n = 0; n < 4; ++n) {
                    acc[n][m] = __builtin_amdgcn_mfma_f32_16x16x32_bf16(fb[slot][n], fa[slot][m], acc[n][m], 0, 0, 0);
                    acc[n][m] = __builtin_amdgcn_mfma_f32_16x16x32_bf16(fb[slot][n], tf, acc[n][m], 0, 0, 0);
                }
            }
            PIN();
        }
        STAMP(3);
#pragma unroll
        for (int m = 0; m < 4; ++m) gl[m] = 2.f * gl[m];
    }
    LBAR();
    {
        char* red = smem + K2_RED + wv * 64 * K2_RS;
#pragma unroll
        for (int m = 0; m < 4; ++m)
#pragma unroll
            for (int n = 0; n < 4; ++n) {
                f32x4 v;
#pragma unroll
                for (int j = 0; j < 4; ++j) v[j] = gl[m] * acc[n][m][j];
                *reinterpret_cast<f32x4*>(red + (m * 16 + fr) * K2_RS + (n * 16 + fq * 4) * 4) = v;
            }
    }
    const int c4 = (tid & 15) * 4;
    const uint2 dv = *reinterpret_cast<const uint2*>(dskip + d0 + c4);
    const uint2 bv = *reinterpret_cast<const uint2*>(b2 + d0 + c4);
    uint2 xv[2]; float gr[2];
#pragma unroll
    for (int i = 0; i < 2; ++i) {
        const int row = (tid >> 4) + 32 * i;
        xv[i] = *reinterpret_cast<const uint2*>(x + (size_t)(l0 + row) * D_ + d0 + c4);
        gr[i] = gate[l0 + row];
    }
    LBAR();
    STAMP(4);
    {
        const float df[4] = {bflo(dv.x), bfhi(dv.x), bflo(dv.y), bfhi(dv.y)};
        const float bf[4] = {bflo(bv.x), bfhi(bv.x), bflo(bv.y), bfhi(bv.y)};
#pragma unroll
        for (int i = 0; i < 2; ++i) {
            const int row = (tid >> 4) + 32 * i;
            const float g = gr[i];
            const float xf[4] = {bflo(xv[i].x), bfhi(xv[i].x), bflo(xv[i].y), bfhi(xv[i].y)};
            f32x4 sacc = *reinterpret_cast<const f32x4*>(smem + K2_RED + row * K2_RS + c4 * 4);
#pragma unroll
            for (int ww = 1; ww < 8; ++ww) sacc += *reinterpret_cast<const f32x4*>(smem + K2_RED + (ww * 64 + row) * K2_RS + c4 * 4);
            f32x4 o;
#pragma unroll
            for (int j = 0; j < 4; ++j) o[j] = sacc[j] + g * xf[j] * df[j] + (1.f - g) * bf[j];
            *reinterpret_cast<f32x4*>(out + (size_t)(l0 + row) * D_ + d0 + c4) = o;
        }
    }
    if (wv == 0 || wv == 5) { STAMP_FLUSH_W(wv ? 3 : 1); }
}

extern "C" void kernel_launch(void* const* d_in, const int* in_sizes, int n_in,
                              void* d_out, int out_size, void* d_ws, size_t ws_size,
                              hipStream_t stream) {
    const bf16* x = (const bf16*)d_in[0];
    const bf16* lre = (const bf16*)d_in[1];
    const bf16* lim = (const bf16*)d_in[2];
    const bf16* bre = (const bf16*)d_in[3];
    const bf16* bim = (const bf16*)d_in[4];
    const bf16* cre = (const bf16*)d_in[5];
    const bf16* cim = (const bf16*)d_in[6];
    const bf16* logdt = (const bf16*)d_in[7];
    const bf16* dskip = (const bf16*)d_in[8];
    const bf16* w1 = (const bf16*)d_in[10];
    const bf16* b1 = (const bf16*)d_in[11];
    const bf16* w2 = (const bf16*)d_in[12];
    const bf16* b2 = (const bf16*)d_in[13];
    const bf16* gw = (const bf16*)d_in[14];

    char* ws = (char*)d_ws;
    char* hloc_t = ws;
    char* H_t = ws + (4u << 20);
    char* w2_t = ws + (12u << 20);
    char* c_t = ws + (14u << 20);
    float* agg = (float*)(ws + (16u << 20));
    float* gate = (float*)(ws + (16u << 20) + 65536);
    float* lamtab = (float*)(ws + (16u << 20) + 65536 + 8192);

    (void)hipFuncSetAttribute((const void*)k1_gemm_scan, hipFuncAttributeMaxDynamicSharedMemorySize, K1_LDS);
    (void)hipFuncSetAttribute((const void*)k2_gemm_mix, hipFuncAttributeMaxDynamicSharedMemorySize, K2_LDS);
    k1_gemm_scan<<<256, 512, K1_LDS, stream>>>(x, lre, lim, bre, bim, logdt, w1, b1, w2, cre, cim, gw, hloc_t, agg, H_t, w2_t, c_t, gate, lamtab);
    k2_gemm_mix<<<256, 512, K2_LDS, stream>>>(x, dskip, b2, hloc_t, agg, H_t, w2_t, c_t, gate, lamtab, (float*)d_out);
}
```

```cpp
#include <hip/hip_runtime.h>
#include <hip/hip_bf16.h>
#include <stdlib.h>

typedef __hip_bfloat16 bf16;
typedef __attribute__((ext_vector_type(8))) short bf16x8;
typedef __attribute__((ext_vector_type(4))) short bf16x4;
typedef __attribute__((ext_vector_type(4))) float f32x4;
typedef __attribute__((ext_vector_type(2))) float f32x2;
typedef __attribute__((ext_vector_type(4))) unsigned u32x4;
typedef __attribute__((ext_vector_type(2))) unsigned u32x2;

constexpr int L_ = 2048, D_ = 512, P_ = 512, F_ = 1792;
constexpr int T_ = 128;
constexpr int NCH = L_ / T_;
constexpr int KSF = F_ / 32;
constexpr int KSS = P_ / 16;

#define WAIT_VM(n) asm volatile("s_waitcnt vmcnt(" #n ")" ::: "memory")
#define BAR() __builtin_amdgcn_s_barrier()
#define LBAR() do { asm volatile("s_waitcnt lgkmcnt(0)" ::: "memory"); __builtin_amdgcn_s_barrier(); asm volatile("" ::: "memory"); } while (0)

#define STAMP_FLUSH_W(k) do {} while (0)
#define STAMP_DECL() do {} while (0)
#define STAMP(i) do {} while (0)
#define STAMP_FLUSH(k) do {} while (0)
#define TR_READ(dst, addr, off) asm volatile("ds_read_b64_tr_b16 %0, %1 offset:%2" : "=v"(dst) : "v"(addr), "i"(off) : "memory")
#define DS_READ128(dst, addr, off) asm volatile("ds_read_b128 %0, %1 offset:%2" : "=v"(dst) : "v"(addr), "i"(off) : "memory")
#define SB0() __builtin_amdgcn_sched_barrier(0)
#define PIN() do { asm volatile("" ::: "memory"); __builtin_amdgcn_sched_barrier(0); } while (0)
#define ST16(p, v) asm volatile("global_store_dwordx4 %0, %1, off sc0 sc1\n\ts_nop 1" :: "v"(p), "v"(v) : "memory")
#define ST8(p, v) asm volatile("global_store_dwordx2 %0, %1, off sc0 sc1\n\ts_nop 1" :: "v"(p), "v"(v) : "memory")

__device__ __forceinline__ float bflo(unsigned u) { return __uint_as_float(u << 16); }
__device__ __forceinline__ float bfhi(unsigned u) { return __uint_as_float(u & 0xffff0000u); }
typedef __attribute__((ext_vector_type(2))) __bf16 bf16x2_t;
__device__ __forceinline__ unsigned pack2(float lo, float hi) {
    f32x2 v = {lo, hi};
    return __builtin_bit_cast(unsigned, __builtin_convertvector(v, bf16x2_t));
}
__device__ __forceinline__ void glds16(const void* g, void* l) {
    __builtin_amdgcn_global_load_lds((const unsigned*)g, (unsigned*)l, 16, 0, 0);
}
__device__ __forceinline__ f32x2 cmul(f32x2 a, f32x2 b) {
    f32x2 r; r.x = a.x * b.x - a.y * b.y; r.y = a.x * b.y + a.y * b.x; return r;
}
__device__ __forceinline__ f32x2 gelu_tanh2(f32x2 v) {
    const float k1 = -2.f * 0.7978845608028654f * 1.4426950408889634f;
    const float k2 = k1 * 0.044715f;
    const f32x2 K1v = {k1, k1}, K2v = {k2, k2}, one = {1.f, 1.f};
    f32x2 a = v * __builtin_elementwise_fma(v * v, K2v, K1v);
    f32x2 e; e.x = __builtin_amdgcn_exp2f(a.x); e.y = __builtin_amdgcn_exp2f(a.y);
    f32x2 d = e + one;
    f32x2 r; r.x = __builtin_amdgcn_rcpf(d.x); r.y = __builtin_amdgcn_rcpf(d.y);
    return v * r;
}

constexpr int K1_A = 128 * 128;
constexpr int K1_BS = 64 * 128;
constexpr int K1_BF = 2 * 64 * 128;
constexpr int K1_STAGE = K1_A + K1_BS + K1_BF;
constexpr int K1_NST = 4;
constexpr int K1_LDS = K1_STAGE * K1_NST;

struct K1Frags { bf16x8 a[2]; bf16x8 s[4]; bf16x4 f[7][2]; };

__global__ __launch_bounds__(512, 2) void k1_gemm_scan(
    const bf16* __restrict__ x, const bf16* __restrict__ lre, const bf16* __restrict__ lim,
    const bf16* __restrict__ bre, const bf16* __restrict__ bim, const bf16* __restrict__ logdt,
    const bf16* __restrict__ w1, const bf16* __restrict__ b1,
    const bf16* __restrict__ w2, const bf16* __restrict__ cre, const bf16* __restrict__ cim,
    const bf16* __restrict__ gw,
    char* __restrict__ hloc_t, float* __restrict__ agg, char* __restrict__ H_t,
    char* __restrict__ w2_t, char* __restrict__ c_t, float* __restrict__ gate, float* __restrict__ lamtab)
{
    extern __shared__ __attribute__((aligned(16))) char smem[];
    const int tid = threadIdx.x, lane = tid & 63;
    const int wv = __builtin_amdgcn_readfirstlane(tid >> 6);
    const int w = wv & 3;
    const int xcd = blockIdx.x & 7, ii = blockIdx.x >> 3;
    const int rp = xcd * 2 + (ii & 1);
    const int cg = ii >> 1;
    const int l0 = rp * T_, p0 = cg * 32, f0 = cg * 112;
    const int fr = lane & 15, fq = lane >> 4;
    STAMP_DECL();
    constexpr int US = 272, USEG = 16 * US + 64;
    char* ubuf = smem;
    char* pbuf = smem + 36864;
    char* hbuf = smem + 40960;

    if (wv >= 4) {
        const int pl = w * 8 + (lane & 7), seg = lane >> 3;
        const bf16* srcA[4]; const bf16* srcS[2]; const bf16* srcF[4];
#pragma unroll
        for (int i = 0; i < 4; ++i) {
            int row = (w * 4 + i) * 8 + (lane >> 3);
            int ch = (lane & 7) ^ ((row >> 1) & 7);
            srcA[i] = x + (size_t)(l0 + row) * D_ + ch * 8;
        }
#pragma unroll
        for (int i = 0; i < 2; ++i) {
            int n = (w * 2 + i) * 8 + (lane >> 3);
            int ch = (lane & 7) ^ ((n >> 1) & 7);
            const bf16* base = (n < 32) ? bre : bim;
            srcS[i] = base + (size_t)(p0 + (n & 31)) * D_ + ch * 8;
        }
#pragma unroll
        for (int i = 0; i < 4; ++i) {
            int ins = w * 4 + i;
            int blk = ins >> 3, r = (ins & 7) * 8 + (lane >> 3);
            int c = lane & 7;
            int fsw = ((r >> 1) & 1) | (((r >> 3) & 1) << 1);
            int col = f0 + blk * 64 + (((c >> 1) ^ fsw) << 4) + (c & 1) * 8;
            if (col > F_ - 8) col = F_ - 8;
            srcF[i] = w1 + (size_t)r * F_ + col;
        }
        const int krot = (rp + cg) & 7;
        auto issue = [&](int kt) {
            char* sb = smem + (kt & (K1_NST - 1)) * K1_STAGE;
            const int ko = ((kt + krot) & 7) * 64;
#pragma unroll
            for (int i = 0; i < 4; ++i) glds16(srcA[i] + ko, sb + (w * 4 + i) * 1024);
#pragma unroll
            for (int i = 0; i < 2; ++i) glds16(srcS[i] + ko, sb + K1_A + (w * 2 + i) * 1024);
#pragma unroll
            for (int i = 0; i < 4; ++i) glds16(srcF[i] + (size_t)ko * F_, sb + K1_A + K1_BS + (w * 4 + i) * 1024);
        };
        STAMP(1);
        issue(0); issue(1);
        WAIT_VM(10);
        STAMP(3);
        BAR();
        issue(2); issue(3);
        STAMP(2);
        asm volatile("" ::: "memory");
#define K1_LSTEP(t, NA)                                       \
        do {                                                  \
            WAIT_VM(NA);                                      \
            STAMP(4 + (t));                                   \
            BAR();                                            \
            if ((t) + 4 < 8) issue((t) + 4);                  \
        } while (0)
        K1_LSTEP(0, 20); K1_LSTEP(1, 20); K1_LSTEP(2, 20); K1_LSTEP(3, 20);
        K1_LSTEP(4, 20); K1_LSTEP(5, 10); K1_LSTEP(6, 0);
#undef K1_LSTEP
        unsigned w2v[2][8];
#pragma unroll
        for (int i = 0; i < 2; ++i) {
            int tl = w + 4 * i; if (tl > 6) tl = 6;
            int tile = blockIdx.x * 7 + tl;
            int nt = tile / KSF, ks = tile - nt * KSF;
            const unsigned short* src = reinterpret_cast<const unsigned short*>(w2) + (size_t)(ks * 32 + fq * 8) * D_ + nt * 16 + fr;
#pragma unroll
            for (int j = 0; j < 8; ++j) asm volatile("global_load_ushort %0, %1, off offset:%2" : "=v"(w2v[i][j]) : "v"(src + (j >> 2) * 4 * D_), "i"((j & 3) * D_ * 2) : "memory");
        }
        u32x2 cvr, cvi;
        {
            int tile = blockIdx.x * 4 + w;
            int nt = tile >> 5, ks = tile & 31;
            size_t o = (size_t)(nt * 16 + fr) * P_ + ks * 16 + fq * 4;
            asm volatile("global_load_dwordx2 %0, %1, off" : "=v"(cvr) : "v"(cre + o) : "memory");
            asm volatile("global_load_dwordx2 %0, %1, off" : "=v"(cvi) : "v"(cim + o) : "memory");
        }
        const int grow = l0 + cg * 8 + w * 2;
        u32x4 gwv, gx0, gx1;
        asm volatile("global_load_dwordx4 %0, %1, off" : "=v"(gwv) : "v"(gw + lane * 8) : "memory");
        asm volatile("global_load_dwordx4 %0, %1, off" : "=v"(gx0) : "v"(x + (size_t)grow * D_ + lane * 8) : "memory");
        asm volatile("global_load_dwordx4 %0, %1, off offset:1024" : "=v"(gx1) : "v"(x + (size_t)grow * D_ + lane * 8) : "memory");
        LBAR();
        LBAR();
        __builtin_amdgcn_s_setprio(3);
        STAMP(11);
        const f32x4 pc = *reinterpret_cast<const f32x4*>(pbuf + pl * 16);
        const f32x2 lam = {pc[0], pc[1]}, coef = {pc[2], pc[3]};
        const f32x2 l2 = cmul(lam, lam), l4 = cmul(l2, l2), l8 = cmul(l4, l4), l16 = cmul(l8, l8);
        {
            f32x2 hv[16]; f32x2 h = {0.f, 0.f};
            const char* up = ubuf + seg * USEG + pl * 8;
#pragma unroll
            for (int i = 0; i < 16; ++i) {
                f32x2 raw = *reinterpret_cast<const f32x2*>(up + i * US);
                f32x2 u = cmul(coef, raw);
                f32x2 t = cmul(lam, h);
                h.x = t.x + u.x; h.y = t.y + u.y; hv[i] = h;
            }
            f32x2 c = h;
            {
                const f32x2 l32 = cmul(l16, l16), l64 = cmul(l32, l32);
                f32x2 o;
                o.x = __shfl_up(c.x, 8); o.y = __shfl_up(c.y, 8);
                if (seg >= 1) { f32x2 t = cmul(l16, o); c.x += t.x; c.y += t.y; }
                o.x = __shfl_up(c.x, 16); o.y = __shfl_up(c.y, 16);
                if (seg >= 2) { f32x2 t = cmul(l32, o); c.x += t.x; c.y += t.y; }
                o.x = __shfl_up(c.x, 32); o.y = __shfl_up(c.y, 32);
                if (seg >= 4) { f32x2 t = cmul(l64, o); c.x += t.x; c.y += t.y; }
            }
            if (seg == 7) { u32x2 v = __builtin_bit_cast(u32x2, c); ST8(agg + ((size_t)rp * P_ + p0 + pl) * 2, v); }
            f32x2 cin; cin.x = __shfl_up(c.x, 8); cin.y = __shfl_up(c.y, 8);
            if (seg == 0) { cin.x = 0.f; cin.y = 0.f; }
            f32x2 t = cmul(cin, lam);
            char* hb = hbuf + w * 4096 + seg * 512 + ((lane >> 2) & 1) * 256 + (lane & 3) * 4;
#pragma unroll
            for (int i = 0; i < 16; ++i) {
                hv[i].x += t.x; hv[i].y += t.y; t = cmul(t, lam);
                *reinterpret_cast<unsigned*>(hb + i * 16) = pack2(hv[i].x, hv[i].y);
            }
            asm volatile("s_waitcnt lgkmcnt(0)" ::: "memory");
            const int ks = cg * 2 + (w >> 1);
#pragma unroll
            for (int i = 0; i < 4; ++i) {
                const int o = i * 1024 + lane * 16;
                const int sg = o >> 9;
                u32x4 v = *reinterpret_cast<const u32x4*>(hbuf + w * 4096 + o);
                char* dst = hloc_t + ((size_t)((rp * 2 + (sg >> 2)) * KSS + ks) * 4 + (sg & 3)) * 1024 + (w & 1) * 512 + (o & 511);
                ST16(dst, v);
            }
        }
        STAMP(12);
        WAIT_VM(5);
        if (rp == 0 && seg == 0) {
            f32x2 l32 = cmul(l16, l16), l64 = cmul(l32, l32), l128 = cmul(l64, l64);
            f32x4 v0 = {lam.x, lam.y, l16.x, l16.y}, v1 = {l64.x, l64.y, l128.x, l128.y};
            ST16(lamtab + (size_t)(p0 + pl) * 8, __builtin_bit_cast(u32x4, v0));
            ST16(lamtab + (size_t)(p0 + pl) * 8 + 4, __builtin_bit_cast(u32x4, v1));
        }
        asm volatile("" : "+v"(w2v[0][0]), "+v"(w2v[0][1]), "+v"(w2v[0][2]), "+v"(w2v[0][3]), "+v"(w2v[0][4]), "+v"(w2v[0][5]), "+v"(w2v[0][6]), "+v"(w2v[0][7]),
                          "+v"(w2v[1][0]), "+v"(w2v[1][1]), "+v"(w2v[1][2]), "+v"(w2v[1][3]), "+v"(w2v[1][4]), "+v"(w2v[1][5]), "+v"(w2v[1][6]), "+v"(w2v[1][7]),
                          "+v"(cvr), "+v"(cvi), "+v"(gwv), "+v"(gx0), "+v"(gx1) :: "memory");
#pragma unroll
        for (int i = 0; i < 2; ++i) {
            int tl = w + 4 * i;
            if (tl < 7) {
                int tile = blockIdx.x * 7 + tl;
                int nt = tile / KSF, ks = tile - nt * KSF;
                u32x4 v;
                v.x = w2v[i][0] | (w2v[i][1] << 16); v.y = w2v[i][2] | (w2v[i][3] << 16);
                v.z = w2v[i][4] | (w2v[i][5] << 16); v.w = w2v[i][6] | (w2v[i][7] << 16);
                ST16(w2_t + ((size_t)((nt >> 2) * KSF + ks) * 4 + (nt & 3)) * 1024 + lane * 16, v);
            }
        }
        {
            int tile = blockIdx.x * 4 + w;
            int nt = tile >> 5, ks = tile & 31;
            unsigned ni0 = cvi.x ^ 0x80008000u, ni1 = cvi.y ^ 0x80008000u;
            u32x4 v;
            v.x = (cvr.x & 0xffffu) | (ni0 << 16); v.y = (cvr.x >> 16) | (ni0 & 0xffff0000u);
            v.z = (cvr.y & 0xffffu) | (ni1 << 16); v.w = (cvr.y >> 16) | (ni1 & 0xffff0000u);
            ST16(c_t + ((size_t)((nt >> 2) * KSS + ks) * 4 + (nt & 3)) * 1024 + lane * 16, v);
        }
        {
            float s0 = bflo(gx0.x) * bflo(gwv.x) + bfhi(gx0.x) * bfhi(gwv.x) + bflo(gx0.y) * bflo(gwv.y) + bfhi(gx0.y) * bfhi(gwv.y)
                     + bflo(gx0.z) * bflo(gwv.z) + bfhi(gx0.z) * bfhi(gwv.z) + bflo(gx0.w) * bflo(gwv.w) + bfhi(gx0.w) * bfhi(gwv.w);
            float s1 = bflo(gx1.x) * bflo(gwv.x) + bfhi(gx1.x) * bfhi(gwv.x) + bflo(gx1.y) * bflo(gwv.y) + bfhi(gx1.y) * bfhi(gwv.y)
                     + bflo(gx1.z) * bflo(gwv.z) + bfhi(gx1.z) * bfhi(gwv.z) + bflo(gx1.w) * bflo(gwv.w) + bfhi(gx1.w) * bfhi(gwv.w);
#pragma unroll
            for (int o = 32; o > 0; o >>= 1) { s0 += __shfl_xor(s0, o); s1 += __shfl_xor(s1, o); }
            if (lane == 0) {
                u32x2 v; v.x = __float_as_uint(1.f / (1.f + __expf(-s0))); v.y = __float_as_uint(1.f / (1.f + __expf(-s1)));
                ST8(gate + grow, v);
            }
        }
        STAMP_FLUSH(2);
        return;
    }

    f32x4 pconst;
    {
        const int p = p0 + w * 8 + (lane & 7);
        const float p_ar = bflo(*reinterpret_cast<const unsigned short*>(lre + p));
        const float p_ai = bflo(*reinterpret_cast<const unsigned short*>(lim + p));
        const float dt = expf(bflo(*reinterpret_cast<const unsigned short*>(logdt + p)));
        float ee = expf(dt * p_ar), sn, cs;
        sincosf(dt * p_ai, &sn, &cs);
        const float lx = ee * cs, ly = ee * sn;
        const float nr = lx - 1.f, ni = ly, den = p_ar * p_ar + p_ai * p_ai;
        pconst = (f32x4){lx, ly, (nr * p_ar + ni * p_ai) / den, (ni * p_ar - nr * p_ai) / den};
    }
    uint2 b1v[7];
#pragma unroll
    for (int n = 0; n < 7; ++n) b1v[n] = *reinterpret_cast<const uint2*>(b1 + f0 + n * 16 + fq * 4);

    f32x4 acc[2][11];
#pragma unroll
    for (int m = 0; m < 2; ++m)
#pragma unroll
        for (int n = 0; n < 11; ++n) {
            if (n < 4) acc[m][n] = (f32x4){0.f, 0.f, 0.f, 0.f};
            else acc[m][n] = (f32x4){bflo(b1v[n - 4].x), bfhi(b1v[n - 4].x), bflo(b1v[n - 4].y), bfhi(b1v[n - 4].y)};
        }

    const unsigned smem_base = (unsigned)(size_t)smem;
    unsigned adA[2], adS[2], adF[2][4];
#pragma unroll
    for (int ks = 0; ks < 2; ++ks) {
        int ch = ks * 4 + fq;
        unsigned lo = fr * 128 + ((ch ^ ((fr >> 1) & 7)) << 4);
        adA[ks] = smem_base + w * 4096 + lo;
        adS[ks] = smem_base + K1_A + lo;
    }
    {
        const int q = (lane & 15) >> 2, pp = lane & 3;
        const int fsw = ((q >> 1) & 1) | ((fq & 1) << 1);
#pragma unroll
        for (int t = 0; t < 2; ++t)
#pragma unroll
            for (int pc = 0; pc < 4; ++pc)
                adF[t][pc] = smem_base + K1_A + K1_BS + (fq * 8 + 4 * t + q) * 128 + ((pc ^ fsw) << 5) + pp * 8;
    }
#define K1_ADV(delta)                                                                       \
    do {                                                                                    \
        _Pragma("unroll") for (int k_ = 0; k_ < 2; ++k_) { adA[k_] += (delta); adS[k_] += (delta); asm volatile("" : "+v"(adA[k_]), "+v"(adS[k_])); } \
        _Pragma("unroll") for (int t_ = 0; t_ < 2; ++t_) _Pragma("unroll") for (int c_ = 0; c_ < 4; ++c_) { adF[t_][c_] += (delta); asm volatile("" : "+v"(adF[t_][c_])); } \
    } while (0)
#define K1_RD(R, i, ks)                                                                     \
    do {                                                                                    \
        if ((i) == 0) DS_READ128(R.a[0], adA[ks], 0);                                       \
        if ((i) == 1) DS_READ128(R.a[1], adA[ks], 2048);                                    \
        if ((i) >= 2 && (i) < 6) DS_READ128(R.s[((i) - 2) & 3], adS[ks], (((i) - 2) & 3) * 2048); \
        if ((i) >= 6 && (i) < 20)                                                           \
            TR_READ(R.f[(((i) - 6) >> 1) % 7][(i) & 1], adF[(i) & 1][(((i) - 6) >> 1) & 3], (((((i) - 6) >> 1) % 7) >> 2) * 8192 + (ks) * 4096); \
    } while (0)
#define K1_WAITL(R)                                                                         \
    asm volatile("s_waitcnt lgkmcnt(0)"                                                     \
        : "+v"(R.a[0]), "+v"(R.a[1]), "+v"(R.s[0]), "+v"(R.s[1]), "+v"(R.s[2]), "+v"(R.s[3]), \
          "+v"(R.f[0][0]), "+v"(R.f[0][1]), "+v"(R.f[1][0]), "+v"(R.f[1][1]),               \
          "+v"(R.f[2][0]), "+v"(R.f[2][1]), "+v"(R.f[3][0]), "+v"(R.f[3][1]),               \
          "+v"(R.f[4][0]), "+v"(R.f[4][1]), "+v"(R.f[5][0]), "+v"(R.f[5][1]),               \
          "+v"(R.f[6][0]), "+v"(R.f[6][1]) :: "memory")
#define K1_MM(R, i)                                                                         \
    do {                                                                                    \
        if ((i) < 8) acc[(i) & 1][((i) >> 1) & 3] = __builtin_amdgcn_mfma_f32_16x16x32_bf16(R.s[((i) >> 1) & 3], R.a[(i) & 1], acc[(i) & 1][((i) >> 1) & 3], 0, 0, 0); \
        else {                                                                              \
            bf16x8 wb = __builtin_shufflevector(R.f[(((i) - 8) >> 1) % 7][0], R.f[(((i) - 8) >> 1) % 7][1], 0, 1, 2, 3, 4, 5, 6, 7); \
            acc[(i) & 1][4 + (((i) - 8) >> 1) % 7] = __builtin_amdgcn_mfma_f32_16x16x32_bf16(wb, R.a[(i) & 1], acc[(i) & 1][4 + (((i) - 8) >> 1) % 7], 0, 0, 0); \
        }                                                                                   \
    } while (0)
    {
        K1Frags R0, R1;
        BAR();
        asm volatile("" ::: "memory");
#pragma unroll
        for (int i = 0; i < 20; ++i) K1_RD(R0, i, 0);
        K1_WAITL(R0);
#define K1_STEP(t)                                            \
        do {                                                  \
            _Pragma("unroll") for (int i = 0; i < 22; ++i) { K1_MM(R0, i); K1_RD(R1, i, 1); SB0(); } \
            K1_WAITL(R1);                                     \
            if ((t) < 7) {                                    \
                K1_ADV((((t) & 3) == 3) ? -3 * K1_STAGE : K1_STAGE); \
                BAR();                                        \
                asm volatile("" ::: "memory");                \
                _Pragma("unroll") for (int i = 0; i < 22; ++i) { K1_MM(R1, i); K1_RD(R0, i, 0); SB0(); } \
                K1_WAITL(R0);                                 \
            } else {                                          \
                BAR();        \
                _Pragma("unroll") for (int i = 0; i < 22; ++i) K1_MM(R1, i); \
            }                                                 \
        } while (0)
        K1_STEP(0); STAMP(1); K1_STEP(1); K1_STEP(2); K1_STEP(3);
        STAMP(2); K1_STEP(4); K1_STEP(5); K1_STEP(6); K1_STEP(7); STAMP(3);
#undef K1_STEP
    }
#pragma unroll
    for (int m = 0; m < 2; ++m)
#pragma unroll
        for (int nt = 0; nt < 2; ++nt) {
            int pq = nt * 16 + fq * 4;
            char* up = ubuf + (w * 2 + m) * USEG + fr * US + pq * 8;
            f32x4 re = acc[m][nt], im = acc[m][nt + 2];
            f32x4 v0 = {re[0], im[0], re[1], im[1]}, v1 = {re[2], im[2], re[3], im[3]};
            *reinterpret_cast<f32x4*>(up) = v0;
            *reinterpret_cast<f32x4*>(up + 16) = v1;
        }
    if (lane < 8) *reinterpret_cast<f32x4*>(pbuf + (w * 8 + lane) * 16) = pconst;
    LBAR();
    STAMP(4);
    {
        char* wimg = smem + 65536 + w * 8192;
        const int fbase = f0 & ~31;
        const int foff = (f0 & 31) ? 512 : 0;
#pragma unroll
        for (int n = 0; n < 7; ++n) {
            const int f = f0 + n * 16 + fq * 4;
            char* hp = wimg + ((f - fbase) >> 5) * 1024 + ((f >> 3) & 3) * 256 + fr * 16 + ((f >> 2) & 1) * 8 - foff;
#pragma unroll
            for (int m = 0; m < 2; ++m) {
                const f32x4 av = acc[m][4 + n];
                const f32x2 o0 = gelu_tanh2((f32x2){av[0], av[1]}), o1 = gelu_tanh2((f32x2){av[2], av[3]});
                u32x2 pk; pk.x = pack2(o0.x, o0.y); pk.y = pack2(o1.x, o1.y);
                *reinterpret_cast<u32x2*>(hp + m * 3584) = pk;
            }
        }
        STAMP(5);
        const int lw = l0 + w * 32;
        char* hrow = H_t + ((size_t)(lw >> 6) * KSF * 4 + ((lw >> 4) & 3) + (size_t)(fbase >> 5) * 4) * 1024;
#pragma unroll
        for (int i = 0; i < 7; ++i) {
            int m, o;
            if (i < 6) { m = i / 3; o = (i % 3) * 1024 + lane * 16; }
            else { m = lane >> 5; o = 3072 + (lane & 31) * 16; }
            u32x4 v = *reinterpret_cast<const u32x4*>(wimg + m * 3584 + o);
            const int og = o + foff;
            ST16(hrow + m * 1024 + (og >> 10) * 4096 + (og & 1023), v);
        }
    }
    STAMP_FLUSH(0);
}

constexpr int K2_RS = 272;
constexpr int K2_SLOT = 18432;
constexpr int K2_LDS = 8 * K2_SLOT;
constexpr int K2_NFW = 4;
constexpr int K2_NFS = 14;
constexpr int K2_NSS = 8;
constexpr int K2_DEPTH = 4;

__global__ __launch_bounds__(512, 2) void k2_gemm_mix(
    const bf16* __restrict__ x, const bf16* __restrict__ dskip, const bf16* __restrict__ b2,
    const char* __restrict__ hloc_t, const float* __restrict__ agg,
    const char* __restrict__ H_t, const char* __restrict__ w2_t, const char* __restrict__ c_t,
    const float* __restrict__ gate, const float* __restrict__ lamtab, float* __restrict__ out)
{
    extern __shared__ __attribute__((aligned(16))) char smem[];
    const int tid = threadIdx.x, lane = tid & 63;
    const int wv = __builtin_amdgcn_readfirstlane(tid >> 6);
    const int xcd = blockIdx.x & 7, ii = blockIdx.x >> 3;
    const int rt = xcd * 4 + (ii & 3);
    const int ct = ii >> 2;
    const int l0 = rt * 64, d0 = ct * 64;
    const int fr = lane & 15, fq = lane >> 4;
    STAMP_DECL();
    const bool is_ffn = wv < K2_NFW;
    const int k0 = is_ffn ? wv : wv - K2_NFW;
    const int kst = is_ffn ? K2_NFW : 8 - K2_NFW;
    const int nst = is_ffn ? (KSF - k0 + kst - 1) / kst : (KSS - k0 + kst - 1) / kst;
    const char* pA = (is_ffn ? H_t + (size_t)(rt * KSF + k0) * 4096 : hloc_t + (size_t)(rt * KSS + k0) * 4096) + lane * 16;
    const char* pB = (is_ffn ? w2_t + (size_t)(ct * KSF + k0) * 4096 : c_t + (size_t)(ct * KSS + k0) * 4096) + lane * 16;
    const int sstride = kst * 4096;

    f32x4 acc[4][4];
#pragma unroll
    for (int n = 0; n < 4; ++n)
#pragma unroll
        for (int m = 0; m < 4; ++m) acc[n][m] = (f32x4){0.f, 0.f, 0.f, 0.f};

    float gl[4];
#pragma unroll
    for (int m = 0; m < 4; ++m) gl[m] = gate[l0 + m * 16 + fr];

    if (is_ffn) {
#define DPF 4
#define DPS 3
        constexpr int DP = DPF;
        bf16x8 fa[DP][4], fb[DP][4];
        auto load_step = [&](int s) {
            const int slot = s % DP;
#pragma unroll
            for (int i = 0; i < 4; ++i) fa[slot][i] = *reinterpret_cast<const bf16x8*>(pA + (size_t)s * sstride + i * 1024);
#pragma unroll
            for (int i = 0; i < 4; ++i) fb[slot][i] = *reinterpret_cast<const bf16x8*>(pB + (size_t)s * sstride + i * 1024);
        };
        PIN();
#pragma unroll
        for (int s = 0; s < DP - 1; ++s) load_step(s);
        PIN();
#pragma unroll
        for (int s = 0; s < K2_NFS; ++s) {
            if (s + DP - 1 < K2_NFS) load_step(s + DP - 1);
            PIN();
            const int slot = s % DP;
#pragma unroll
            for (int n = 0; n < 4; ++n)
#pragma unroll
                for (int m = 0; m < 4; ++m) acc[n][m] = __builtin_amdgcn_mfma_f32_16x16x32_bf16(fb[slot][n], fa[slot][m], acc[n][m], 0, 0, 0);
            PIN();
        }
        STAMP(3);
#pragma unroll
        for (int m = 0; m < 4; ++m) gl[m] = 1.f - gl[m];
    } else {
        __builtin_amdgcn_s_setprio(2);
        constexpr int DP = DPS;
        bf16x8 fa[DP][4], fb[DP][4];
        auto load_step = [&](int s) {
            const int slot = s % DP;
#pragma unroll
            for (int i = 0; i < 4; ++i) fa[slot][i] = *reinterpret_cast<const bf16x8*>(pA + (size_t)s * sstride + i * 1024);
#pragma unroll
            for (int i = 0; i < 4; ++i) fb[slot][i] = *reinterpret_cast<const bf16x8*>(pB + (size_t)s * sstride + i * 1024);
        };
        char* T1w = smem + wv * K2_SLOT;
        char* L16w = T1w + 17408;
        {
            const int chunk = rt >> 1, jbase = (rt & 1) * 64 + 1;
            f32x4 lt0[2], lt1[2]; f32x2 ag[2][NCH - 1];
#pragma unroll
            for (int h = 0; h < 2; ++h) {
                const int q = lane + h * 64;
                const int p = ((q >> 4) * 4 + k0) * 16 + (q & 15);
                lt0[h] = *reinterpret_cast<const f32x4*>(lamtab + (size_t)p * 8);
                lt1[h] = *reinterpret_cast<const f32x4*>(lamtab + (size_t)p * 8 + 4);
#pragma unroll
                for (int c = 0; c < NCH - 1; ++c) ag[h][c] = *reinterpret_cast<const f32x2*>(agg + ((size_t)c * P_ + p) * 2);
            }
            PIN();
            load_step(0);
            PIN();
#pragma unroll
            for (int h = 0; h < 2; ++h) {
                const int q = lane + h * 64;
                const f32x2 lam = {lt0[h][0], lt0[h][1]}, l16 = {lt0[h][2], lt0[h][3]};
                const f32x2 l64 = {lt1[h][0], lt1[h][1]}, l128 = {lt1[h][2], lt1[h][3]};
                f32x2 S = {0.f, 0.f};
#pragma unroll
                for (int c = 0; c < NCH - 1; ++c) {
                    if (c < chunk) { f32x2 t = cmul(l128, S); S.x = t.x + ag[h][c].x; S.y = t.y + ag[h][c].y; }
                }
                f32x2 t = cmul(S, lam);
                if (jbase > 1) t = cmul(t, l64);
#pragma unroll
                for (int r = 0; r < 16; ++r) { *reinterpret_cast<f32x2*>(T1w + (r * 128 + q) * 8 + r * 16) = t; t = cmul(t, lam); }
                *reinterpret_cast<f32x2*>(L16w + q * 8) = l16;
            }
        }
        STAMP(1);
        PIN();
#pragma unroll
        for (int s = 1; s < DP - 1; ++s) load_step(s);
        PIN();
        asm volatile("s_waitcnt lgkmcnt(0)" ::: "memory");
        STAMP(2);
        const char* T1b = T1w + (fr * 128 + fq * 4) * 8 + fr * 16;
        const char* L16b = L16w + (fq * 4) * 8;
#pragma unroll
        for (int s = 0; s < K2_NSS; ++s) {
            if (s + DP - 1 < K2_NSS) load_step(s + DP - 1);
            PIN();
            const int slot = s % DP;
            const int pofs = s * 16 * 8;
            f32x4 t01 = *reinterpret_cast<const f32x4*>(T1b + pofs), t23 = *reinterpret_cast<const f32x4*>(T1b + pofs + 16);
            f32x4 m01 = *reinterpret_cast<const f32x4*>(L16b + pofs), m23 = *reinterpret_cast<const f32x4*>(L16b + pofs + 16);
            f32x2 t[4] = {{t01[0], t01[1]}, {t01[2], t01[3]}, {t23[0], t23[1]}, {t23[2], t23[3]}};
            const f32x2 mm[4] = {{m01[0], m01[1]}, {m01[2], m01[3]}, {m23[0], m23[1]}, {m23[2], m23[3]}};
#pragma unroll
            for (int m = 0; m < 4; ++m) {
                u32x4 o;
#pragma unroll
                for (int j = 0; j < 4; ++j) {
                    o[j] = pack2(t[j].x, t[j].y);
                    if (m < 3) t[j] = cmul(t[j], mm[j]);
                }
                const bf16x8 tf = __builtin_bit_cast(bf16x8, o);
#pragma unroll
                for (int n = 0; n < 4; ++n) {
                    acc[n][m] = __builtin_amdgcn_mfma_f32_16x16x32_bf16(fb[slot][n], fa[slot][m], acc[n][m], 0, 0, 0);
                    acc[n][m] = __builtin_amdgcn_mfma_f32_16x16x32_bf16(fb[slot][n], tf, acc[n][m], 0, 0, 0);
                }
            }
            PIN();
        }
        STAMP(3);
#pragma unroll
        for (int m = 0; m < 4; ++m) gl[m] = 2.f * gl[m];
    }
    {
        char* red = smem + wv * K2_SLOT;
#pragma unroll
        for (int m = 0; m < 4; ++m)
#pragma unroll
            for (int n = 0; n < 4; ++n) {
                f32x4 v;
#pragma unroll
                for (int j = 0; j < 4; ++j) v[j] = gl[m] * acc[n][m][j];
                *reinterpret_cast<f32x4*>(red + (m * 16 + fr) * K2_RS + (n * 16 + fq * 4) * 4) = v;
            }
    }
    const int c4 = (tid & 15) * 4;
    const uint2 dv = *reinterpret_cast<const uint2*>(dskip + d0 + c4);
    const uint2 bv = *reinterpret_cast<const uint2*>(b2 + d0 + c4);
    uint2 xv[2]; float gr[2];
#pragma unroll
    for (int i = 0; i < 2; ++i) {
        const int row = (tid >> 4) + 32 * i;
        xv[i] = *reinterpret_cast<const uint2*>(x + (size_t)(l0 + row) * D_ + d0 + c4);
        gr[i] = gate[l0 + row];
    }
    LBAR();
    STAMP(4);
    {
        const float df[4] = {bflo(dv.x), bfhi(dv.x), bflo(dv.y), bfhi(dv.y)};
        const float bf[4] = {bflo(bv.x), bfhi(bv.x), bflo(bv.y), bfhi(bv.y)};
#pragma unroll
        for (int i = 0; i < 2; ++i) {
            const int row = (tid >> 4) + 32 * i;
            const float g = gr[i];
            const float xf[4] = {bflo(xv[i].x), bfhi(xv[i].x), bflo(xv[i].y), bfhi(xv[i].y)};
            f32x4 sacc = *reinterpret_cast<const f32x4*>(smem + row * K2_RS + c4 * 4);
#pragma unroll
            for (int ww = 1; ww < 8; ++ww) sacc += *reinterpret_cast<const f32x4*>(smem + ww * K2_SLOT + row * K2_RS + c4 * 4);
            f32x4 o;
#pragma unroll
            for (int j = 0; j < 4; ++j) o[j] = sacc[j] + g * xf[j] * df[j] + (1.f - g) * bf[j];
            *reinterpret_cast<f32x4*>(out + (size_t)(l0 + row) * D_ + d0 + c4) = o;
        }
    }
    if (wv == 0 || wv == 5) { STAMP_FLUSH_W(wv ? 3 : 1); }
}

extern "C" void kernel_launch(void* const* d_in, const int* in_sizes, int n_in,
                              void* d_out, int out_size, void* d_ws, size_t ws_size,
                              hipStream_t stream) {
    const bf16* x = (const bf16*)d_in[0];
    const bf16* lre = (const bf16*)d_in[1];
    const bf16* lim = (const bf16*)d_in[2];
    const bf16* bre = (const bf16*)d_in[3];
    const bf16* bim = (const bf16*)d_in[4];
    const bf16* cre = (const bf16*)d_in[5];
    const bf16* cim = (const bf16*)d_in[6];
    const bf16* logdt = (const bf16*)d_in[7];
    const bf16* dskip = (const bf16*)d_in[8];
    const bf16* w1 = (const bf16*)d_in[10];
    const bf16* b1 = (const bf16*)d_in[11];
    const bf16* w2 = (const bf16*)d_in[12];
    const bf16* b2 = (const bf16*)d_in[13];
    const bf16* gw = (const bf16*)d_in[14];

    char* ws = (char*)d_ws;
    char* hloc_t = ws;
    char* H_t = ws + (4u << 20);
    char* w2_t = ws + (12u << 20);
    char* c_t = ws + (14u << 20);
    float* agg = (float*)(ws + (16u << 20));
    float* gate = (float*)(ws + (16u << 20) + 65536);
    float* lamtab = (float*)(ws + (16u << 20) + 65536 + 8192);

    (void)hipFuncSetAttribute((const void*)k1_gemm_scan, hipFuncAttributeMaxDynamicSharedMemorySize, K1_LDS);
    (void)hipFuncSetAttribute((const void*)k2_gemm_mix, hipFuncAttributeMaxDynamicSharedMemorySize, K2_LDS);
    k1_gemm_scan<<<256, 512, K1_LDS, stream>>>(x, lre, lim, bre, bim, logdt, w1, b1, w2, cre, cim, gw, hloc_t, agg, H_t, w2_t, c_t, gate, lamtab);
    k2_gemm_mix<<<256, 512, K2_LDS, stream>>>(x, dskip, b2, hloc_t, agg, H_t, w2_t, c_t, gate, lamtab, (float*)d_out);
}
```

```cpp
#include <hip/hip_runtime.h>
#include <hip/hip_bf16.h>

typedef __hip_bfloat16 bf16;
typedef __attribute__((ext_vector_type(8))) short bf16x8;
typedef __attribute__((ext_vector_type(4))) short bf16x4;
typedef __attribute__((ext_vector_type(4))) float f32x4;
typedef __attribute__((ext_vector_type(2))) float f32x2;
typedef __attribute__((ext_vector_type(4))) unsigned u32x4;
typedef __attribute__((ext_vector_type(2))) unsigned u32x2;

constexpr int L_ = 2048, D_ = 512, P_ = 512, F_ = 1792;
constexpr int T_ = 128;
constexpr int NCH = L_ / T_;
constexpr int KSF = F_ / 32;
constexpr int KSS = P_ / 16;

#define WAIT_VM(n) asm volatile("s_waitcnt vmcnt(" #n ")" ::: "memory")
#define BAR() __builtin_amdgcn_s_barrier()
#define LBAR() do { asm volatile("s_waitcnt lgkmcnt(0)" ::: "memory"); __builtin_amdgcn_s_barrier(); asm volatile("" ::: "memory"); } while (0)

#define STAMP_FLUSH_W(k) do {} while (0)
#define STAMP_DECL() do {} while (0)
#define STAMP(i) do {} while (0)
#define STAMP_FLUSH(k) do {} while (0)
#define TR_READ(dst, addr, off) asm volatile("ds_read_b64_tr_b16 %0, %1 offset:%2" : "=v"(dst) : "v"(addr), "i"(off) : "memory")
#define DS_READ128(dst, addr, off) asm volatile("ds_read_b128 %0, %1 offset:%2" : "=v"(dst) : "v"(addr), "i"(off) : "memory")
#define SB0() __builtin_amdgcn_sched_barrier(0)
#define PIN() do { asm volatile("" ::: "memory"); __builtin_amdgcn_sched_barrier(0); } while (0)
#define ST16(p, v) asm volatile("global_store_dwordx4 %0, %1, off sc0 sc1\n\ts_nop 1" :: "v"(p), "v"(v) : "memory")
#define ST8(p, v) asm volatile("global_store_dwordx2 %0, %1, off sc0 sc1\n\ts_nop 1" :: "v"(p), "v"(v) : "memory")

__device__ __forceinline__ float bflo(unsigned u) { return __uint_as_float(u << 16); }
__device__ __forceinline__ float bfhi(unsigned u) { return __uint_as_float(u & 0xffff0000u); }
typedef __attribute__((ext_vector_type(2))) __bf16 bf16x2_t;
__device__ __forceinline__ unsigned pack2(float lo, float hi) {
    f32x2 v = {lo, hi};
    return __builtin_bit_cast(unsigned, __builtin_convertvector(v, bf16x2_t));
}
__device__ __forceinline__ void glds16(const void* g, void* l) {
    __builtin_amdgcn_global_load_lds((const unsigned*)g, (unsigned*)l, 16, 0, 0);
}
__device__ __forceinline__ f32x2 cmul(f32x2 a, f32x2 b) {
    f32x2 r; r.x = a.x * b.x - a.y * b.y; r.y = a.x * b.y + a.y * b.x; return r;
}
__device__ __forceinline__ f32x2 gelu_tanh2(f32x2 v) {
    const float k1 = -2.f * 0.7978845608028654f * 1.4426950408889634f;
    const float k2 = k1 * 0.044715f;
    const f32x2 K1v = {k1, k1}, K2v = {k2, k2}, one = {1.f, 1.f};
    f32x2 a = v * __builtin_elementwise_fma(v * v, K2v, K1v);
    f32x2 e; e.x = __builtin_amdgcn_exp2f(a.x); e.y = __builtin_amdgcn_exp2f(a.y);
    f32x2 d = e + one;
    f32x2 r; r.x = __builtin_amdgcn_rcpf(d.x); r.y = __builtin_amdgcn_rcpf(d.y);
    return v * r;
}

constexpr int K1_A = 128 * 128;
constexpr int K1_BS = 64 * 128;
constexpr int K1_BF = 2 * 64 * 128;
constexpr int K1_STAGE = K1_A + K1_BS + K1_BF;
constexpr int K1_NST = 4;
constexpr int K1_LDS = K1_STAGE * K1_NST;

struct K1Frags { bf16x8 a[2]; bf16x8 s[4]; bf16x4 f[7][2]; };

__global__ __launch_bounds__(512, 2) void k1_gemm_scan(
    const bf16* __restrict__ x, const bf16* __restrict__ lre, const bf16* __restrict__ lim,
    const bf16* __restrict__ bre, const bf16* __restrict__ bim, const bf16* __restrict__ logdt,
    const bf16* __restrict__ w1, const bf16* __restrict__ b1,
    const bf16* __restrict__ w2, const bf16* __restrict__ cre, const bf16* __restrict__ cim,
    const bf16* __restrict__ gw,
    char* __restrict__ hloc_t, float* __restrict__ agg, char* __restrict__ H_t,
    char* __restrict__ w2_t, char* __restrict__ c_t, float* __restrict__ gate, float* __restrict__ lamtab)
{
    extern __shared__ __attribute__((aligned(16))) char smem[];
    const int tid = threadIdx.x, lane = tid & 63;
    const int wv = __builtin_amdgcn_readfirstlane(tid >> 6);
    const int w = wv & 3;
    const int xcd = blockIdx.x & 7, ii = blockIdx.x >> 3;
    const int rp = xcd * 2 + (ii & 1);
    const int cg = ii >> 1;
    const int l0 = rp * T_, p0 = cg * 32, f0 = cg * 112;
    const int fr = lane & 15, fq = lane >> 4;
    STAMP_DECL();
    constexpr int US = 272, USEG = 16 * US + 64;
    char* ubuf = smem;
    char* pbuf = smem + 36864;
    char* hbuf = smem + 40960;

    if (wv >= 4) {
        const int pl = w * 8 + (lane & 7), seg = lane >> 3;
        const bf16* srcA[4]; const bf16* srcS[2]; const bf16* srcF[4];
#pragma unroll
        for (int i = 0; i < 4; ++i) {
            int row = (w * 4 + i) * 8 + (lane >> 3);
            int ch = (lane & 7) ^ ((row >> 1) & 7);
            srcA[i] = x + (size_t)(l0 + row) * D_ + ch * 8;
        }
#pragma unroll
        for (int i = 0; i < 2; ++i) {
            int n = (w * 2 + i) * 8 + (lane >> 3);
            int ch = (lane & 7) ^ ((n >> 1) & 7);
            const bf16* base = (n < 32) ? bre : bim;
            srcS[i] = base + (size_t)(p0 + (n & 31)) * D_ + ch * 8;
        }
#pragma unroll
        for (int i = 0; i < 4; ++i) {
            int ins = w * 4 + i;
            int blk = ins >> 3, r = (ins & 7) * 8 + (lane >> 3);
            int c = lane & 7;
            int fsw = ((r >> 1) & 1) | (((r >> 3) & 1) << 1);
            int col = f0 + blk * 64 + (((c >> 1) ^ fsw) << 4) + (c & 1) * 8;
            if (col > F_ - 8) col = F_ - 8;
            srcF[i] = w1 + (size_t)r * F_ + col;
        }
        const int krot = (rp + cg) & 7;
        auto issue = [&](int kt) {
            char* sb = smem + (kt & (K1_NST - 1)) * K1_STAGE;
            const int ko = ((kt + krot) & 7) * 64;
#pragma unroll
            for (int i = 0; i < 4; ++i) glds16(srcA[i] + ko, sb + (w * 4 + i) * 1024);
#pragma unroll
            for (int i = 0; i < 2; ++i) glds16(srcS[i] + ko, sb + K1_A + (w * 2 + i) * 1024);
#pragma unroll
            for (int i = 0; i < 4; ++i) glds16(srcF[i] + (size_t)ko * F_, sb + K1_A + K1_BS + (w * 4 + i) * 1024);
        };
        STAMP(1);
        issue(0); issue(1);
        WAIT_VM(10);
        STAMP(3);
        BAR();
        issue(2); issue(3);
        STAMP(2);
        asm volatile("" ::: "memory");
#define K1_LSTEP(t, NA)                                       \
        do {                                                  \
            WAIT_VM(NA);                                      \
            STAMP(4 + (t));                                   \
            BAR();                                            \
            if ((t) + 4 < 8) issue((t) + 4);                  \
        } while (0)
        K1_LSTEP(0, 20); K1_LSTEP(1, 20); K1_LSTEP(2, 20); K1_LSTEP(3, 20);
        K1_LSTEP(4, 20); K1_LSTEP(5, 10); K1_LSTEP(6, 0);
#undef K1_LSTEP
        unsigned w2v[2][8];
#pragma unroll
        for (int i = 0; i < 2; ++i) {
            int tl = w + 4 * i; if (tl > 6) tl = 6;
            int tile = blockIdx.x * 7 + tl;
            int nt = tile / KSF, ks = tile - nt * KSF;
            const unsigned short* src = reinterpret_cast<const unsigned short*>(w2) + (size_t)(ks * 32 + fq * 8) * D_ + nt * 16 + fr;
#pragma unroll
            for (int j = 0; j < 8; ++j) asm volatile("global_load_ushort %0, %1, off offset:%2" : "=v"(w2v[i][j]) : "v"(src + (j >> 2) * 4 * D_), "i"((j & 3) * D_ * 2) : "memory");
        }
        u32x2 cvr, cvi;
        {
            int tile = blockIdx.x * 4 + w;
            int nt = tile >> 5, ks = tile & 31;
            size_t o = (size_t)(nt * 16 + fr) * P_ + ks * 16 + fq * 4;
            asm volatile("global_load_dwordx2 %0, %1, off" : "=v"(cvr) : "v"(cre + o) : "memory");
            asm volatile("global_load_dwordx2 %0, %1, off" : "=v"(cvi) : "v"(cim + o) : "memory");
        }
        const int grow = l0 + cg * 8 + w * 2;
        u32x4 gwv, gx0, gx1;
        asm volatile("global_load_dwordx4 %0, %1, off" : "=v"(gwv) : "v"(gw + lane * 8) : "memory");
        asm volatile("global_load_dwordx4 %0, %1, off" : "=v"(gx0) : "v"(x + (size_t)grow * D_ + lane * 8) : "memory");
        asm volatile("global_load_dwordx4 %0, %1, off offset:1024" : "=v"(gx1) : "v"(x + (size_t)grow * D_ + lane * 8) : "memory");
        LBAR();
        LBAR();
        __builtin_amdgcn_s_setprio(3);
        STAMP(11);
        const f32x4 pc = *reinterpret_cast<const f32x4*>(pbuf + pl * 16);
        const f32x2 lam = {pc[0], pc[1]}, coef = {pc[2], pc[3]};
        const f32x2 l2 = cmul(lam, lam), l4 = cmul(l2, l2), l8 = cmul(l4, l4), l16 = cmul(l8, l8);
        {
            f32x2 hv[16]; f32x2 h = {0.f, 0.f};
            const char* up = ubuf + seg * USEG + pl * 8;
#pragma unroll
            for (int i = 0; i < 16; ++i) {
                f32x2 raw = *reinterpret_cast<const f32x2*>(up + i * US);
                f32x2 u = cmul(coef, raw);
                f32x2 t = cmul(lam, h);
                h.x = t.x + u.x; h.y = t.y + u.y; hv[i] = h;
            }
            f32x2 c = h;
            {
                const f32x2 l32 = cmul(l16, l16), l64 = cmul(l32, l32);
                f32x2 o;
                o.x = __shfl_up(c.x, 8); o.y = __shfl_up(c.y, 8);
                if (seg >= 1) { f32x2 t = cmul(l16, o); c.x += t.x; c.y += t.y; }
                o.x = __shfl_up(c.x, 16); o.y = __shfl_up(c.y, 16);
                if (seg >= 2) { f32x2 t = cmul(l32, o); c.x += t.x; c.y += t.y; }
                o.x = __shfl_up(c.x, 32); o.y = __shfl_up(c.y, 32);
                if (seg >= 4) { f32x2 t = cmul(l64, o); c.x += t.x; c.y += t.y; }
            }
            if (seg == 7) { u32x2 v = __builtin_bit_cast(u32x2, c); ST8(agg + ((size_t)rp * P_ + p0 + pl) * 2, v); }
            f32x2 cin; cin.x = __shfl_up(c.x, 8); cin.y = __shfl_up(c.y, 8);
            if (seg == 0) { cin.x = 0.f; cin.y = 0.f; }
            f32x2 t = cmul(cin, lam);
            char* hb = hbuf + w * 4096 + seg * 512 + ((lane >> 2) & 1) * 256 + (lane & 3) * 4;
#pragma unroll
            for (int i = 0; i < 16; ++i) {
                hv[i].x += t.x; hv[i].y += t.y; t = cmul(t, lam);
                *reinterpret_cast<unsigned*>(hb + i * 16) = pack2(hv[i].x, hv[i].y);
            }
            asm volatile("s_waitcnt lgkmcnt(0)" ::: "memory");
            const int ks = cg * 2 + (w >> 1);
#pragma unroll
            for (int i = 0; i < 4; ++i) {
                const int o = i * 1024 + lane * 16;
                const int sg = o >> 9;
                u32x4 v = *reinterpret_cast<const u32x4*>(hbuf + w * 4096 + o);
                char* dst = hloc_t + ((size_t)((rp * 2 + (sg >> 2)) * KSS + ks) * 4 + (sg & 3)) * 1024 + (w & 1) * 512 + (o & 511);
                ST16(dst, v);
            }
        }
        STAMP(12);
        WAIT_VM(5);
        if (rp == 0 && seg == 0) {
            f32x2 l32 = cmul(l16, l16), l64 = cmul(l32, l32), l128 = cmul(l64, l64);
            f32x4 v0 = {lam.x, lam.y, l16.x, l16.y}, v1 = {l64.x, l64.y, l128.x, l128.y};
            ST16(lamtab + (size_t)(p0 + pl) * 8, __builtin_bit_cast(u32x4, v0));
            ST16(lamtab + (size_t)(p0 + pl) * 8 + 4, __builtin_bit_cast(u32x4, v1));
        }
        asm volatile("" : "+v"(w2v[0][0]), "+v"(w2v[0][1]), "+v"(w2v[0][2]), "+v"(w2v[0][3]), "+v"(w2v[0][4]), "+v"(w2v[0][5]), "+v"(w2v[0][6]), "+v"(w2v[0][7]),
                          "+v"(w2v[1][0]), "+v"(w2v[1][1]), "+v"(w2v[1][2]), "+v"(w2v[1][3]), "+v"(w2v[1][4]), "+v"(w2v[1][5]), "+v"(w2v[1][6]), "+v"(w2v[1][7]),
                          "+v"(cvr), "+v"(cvi), "+v"(gwv), "+v"(gx0), "+v"(gx1) :: "memory");
#pragma unroll
        for (int i = 0; i < 2; ++i) {
            int tl = w + 4 * i;
            if (tl < 7) {
                int tile = blockIdx.x * 7 + tl;
                int nt = tile / KSF, ks = tile - nt * KSF;
                u32x4 v;
                v.x = w2v[i][0] | (w2v[i][1] << 16); v.y = w2v[i][2] | (w2v[i][3] << 16);
                v.z = w2v[i][4] | (w2v[i][5] << 16); v.w = w2v[i][6] | (w2v[i][7] << 16);
                ST16(w2_t + ((size_t)((nt >> 2) * KSF + ks) * 4 + (nt & 3)) * 1024 + lane * 16, v);
            }
        }
        {
            int tile = blockIdx.x * 4 + w;
            int nt = tile >> 5, ks = tile & 31;
            unsigned ni0 = cvi.x ^ 0x80008000u, ni1 = cvi.y ^ 0x80008000u;
            u32x4 v;
            v.x = (cvr.x & 0xffffu) | (ni0 << 16); v.y = (cvr.x >> 16) | (ni0 & 0xffff0000u);
            v.z = (cvr.y & 0xffffu) | (ni1 << 16); v.w = (cvr.y >> 16) | (ni1 & 0xffff0000u);
            ST16(c_t + ((size_t)((nt >> 2) * KSS + ks) * 4 + (nt & 3)) * 1024 + lane * 16, v);
        }
        {
            float s0 = bflo(gx0.x) * bflo(gwv.x) + bfhi(gx0.x) * bfhi(gwv.x) + bflo(gx0.y) * bflo(gwv.y) + bfhi(gx0.y) * bfhi(gwv.y)
                     + bflo(gx0.z) * bflo(gwv.z) + bfhi(gx0.z) * bfhi(gwv.z) + bflo(gx0.w) * bflo(gwv.w) + bfhi(gx0.w) * bfhi(gwv.w);
            float s1 = bflo(gx1.x) * bflo(gwv.x) + bfhi(gx1.x) * bfhi(gwv.x) + bflo(gx1.y) * bflo(gwv.y) + bfhi(gx1.y) * bfhi(gwv.y)
                     + bflo(gx1.z) * bflo(gwv.z) + bfhi(gx1.z) * bfhi(gwv.z) + bflo(gx1.w) * bflo(gwv.w) + bfhi(gx1.w) * bfhi(gwv.w);
#pragma unroll
            for (int o = 32; o > 0; o >>= 1) { s0 += __shfl_xor(s0, o); s1 += __shfl_xor(s1, o); }
            if (lane == 0) {
                u32x2 v; v.x = __float_as_uint(1.f / (1.f + __expf(-s0))); v.y = __float_as_uint(1.f / (1.f + __expf(-s1)));
                ST8(gate + grow, v);
            }
        }
        STAMP_FLUSH(2);
        return;
    }

    f32x4 pconst;
    {
        const int p = p0 + w * 8 + (lane & 7);
        const float p_ar = bflo(*reinterpret_cast<const unsigned short*>(lre + p));
        const float p_ai = bflo(*reinterpret_cast<const unsigned short*>(lim + p));
        const float dt = expf(bflo(*reinterpret_cast<const unsigned short*>(logdt + p)));
        float ee = expf(dt * p_ar), sn, cs;
        sincosf(dt * p_ai, &sn, &cs);
        const float lx = ee * cs, ly = ee * sn;
        const float nr = lx - 1.f, ni = ly, den = p_ar * p_ar + p_ai * p_ai;
        pconst = (f32x4){lx, ly, (nr * p_ar + ni * p_ai) / den, (ni * p_ar - nr * p_ai) / den};
    }
    uint2 b1v[7];
#pragma unroll
    for (int n = 0; n < 7; ++n) b1v[n] = *reinterpret_cast<const uint2*>(b1 + f0 + n * 16 + fq * 4);

    f32x4 acc[2][11];
#pragma unroll
    for (int m = 0; m < 2; ++m)
#pragma unroll
        for (int n = 0; n < 11; ++n) {
            if (n < 4) acc[m][n] = (f32x4){0.f, 0.f, 0.f, 0.f};
            else acc[m][n] = (f32x4){bflo(b1v[n - 4].x), bfhi(b1v[n - 4].x), bflo(b1v[n - 4].y), bfhi(b1v[n - 4].y)};
        }

    const unsigned smem_base = (unsigned)(size_t)smem;
    unsigned adA[2], adS[2], adF[2][4];
#pragma unroll
    for (int ks = 0; ks < 2; ++ks) {
        int ch = ks * 4 + fq;
        unsigned lo = fr * 128 + ((ch ^ ((fr >> 1) & 7)) << 4);
        adA[ks] = smem_base + w * 4096 + lo;
        adS[ks] = smem_base + K1_A + lo;
    }
    {
        const int q = (lane & 15) >> 2, pp = lane & 3;
        const int fsw = ((q >> 1) & 1) | ((fq & 1) << 1);
#pragma unroll
        for (int t = 0; t < 2; ++t)
#pragma unroll
            for (int pc = 0; pc < 4; ++pc)
                adF[t][pc] = smem_base + K1_A + K1_BS + (fq * 8 + 4 * t + q) * 128 + ((pc ^ fsw) << 5) + pp * 8;
    }
#define K1_ADV(delta)                                                                       \
    do {                                                                                    \
        _Pragma("unroll") for (int k_ = 0; k_ < 2; ++k_) { adA[k_] += (delta); adS[k_] += (delta); asm volatile("" : "+v"(adA[k_]), "+v"(adS[k_])); } \
        _Pragma("unroll") for (int t_ = 0; t_ < 2; ++t_) _Pragma("unroll") for (int c_ = 0; c_ < 4; ++c_) { adF[t_][c_] += (delta); asm volatile("" : "+v"(adF[t_][c_])); } \
    } while (0)
#define K1_RD(R, i, ks)                                                                     \
    do {                                                                                    \
        if ((i) == 0) DS_READ128(R.a[0], adA[ks], 0);                                       \
        if ((i) == 1) DS_READ128(R.a[1], adA[ks], 2048);                                    \
        if ((i) >= 2 && (i) < 6) DS_READ128(R.s[((i) - 2) & 3], adS[ks], (((i) - 2) & 3) * 2048); \
        if ((i) >= 6 && (i) < 20)                                                           \
            TR_READ(R.f[(((i) - 6) >> 1) % 7][(i) & 1], adF[(i) & 1][(((i) - 6) >> 1) & 3], (((((i) - 6) >> 1) % 7) >> 2) * 8192 + (ks) * 4096); \
    } while (0)
#define K1_WAITL(R)                                                                         \
    asm volatile("s_waitcnt lgkmcnt(0)"                                                     \
        : "+v"(R.a[0]), "+v"(R.a[1]), "+v"(R.s[0]), "+v"(R.s[1]), "+v"(R.s[2]), "+v"(R.s[3]), \
          "+v"(R.f[0][0]), "+v"(R.f[0][1]), "+v"(R.f[1][0]), "+v"(R.f[1][1]),               \
          "+v"(R.f[2][0]), "+v"(R.f[2][1]), "+v"(R.f[3][0]), "+v"(R.f[3][1]),               \
          "+v"(R.f[4][0]), "+v"(R.f[4][1]), "+v"(R.f[5][0]), "+v"(R.f[5][1]),               \
          "+v"(R.f[6][0]), "+v"(R.f[6][1]) :: "memory")
#define K1_MM(R, i)                                                                         \
    do {                                                                                    \
        if ((i) < 8) acc[(i) & 1][((i) >> 1) & 3] = __builtin_amdgcn_mfma_f32_16x16x32_bf16(R.s[((i) >> 1) & 3], R.a[(i) & 1], acc[(i) & 1][((i) >> 1) & 3], 0, 0, 0); \
        else {                                                                              \
            bf16x8 wb = __builtin_shufflevector(R.f[(((i) - 8) >> 1) % 7][0], R.f[(((i) - 8) >> 1) % 7][1], 0, 1, 2, 3, 4, 5, 6, 7); \
            acc[(i) & 1][4 + (((i) - 8) >> 1) % 7] = __builtin_amdgcn_mfma_f32_16x16x32_bf16(wb, R.a[(i) & 1], acc[(i) & 1][4 + (((i) - 8) >> 1) % 7], 0, 0, 0); \
        }                                                                                   \
    } while (0)
    {
        K1Frags R0, R1;
        BAR();
        asm volatile("" ::: "memory");
#pragma unroll
        for (int i = 0; i < 20; ++i) K1_RD(R0, i, 0);
        K1_WAITL(R0);
#define K1_STEP(t)                                            \
        do {                                                  \
            _Pragma("unroll") for (int i = 0; i < 22; ++i) { K1_MM(R0, i); K1_RD(R1, i, 1); SB0(); } \
            K1_WAITL(R1);                                     \
            if ((t) < 7) {                                    \
                K1_ADV((((t) & 3) == 3) ? -3 * K1_STAGE : K1_STAGE); \
                BAR();                                        \
                asm volatile("" ::: "memory");                \
                _Pragma("unroll") for (int i = 0; i < 22; ++i) { K1_MM(R1, i); K1_RD(R0, i, 0); SB0(); } \
                K1_WAITL(R0);                                 \
            } else {                                          \
                BAR();        \
                _Pragma("unroll") for (int i = 0; i < 22; ++i) K1_MM(R1, i); \
            }                                                 \
        } while (0)
        K1_STEP(0); STAMP(1); K1_STEP(1); K1_STEP(2); K1_STEP(3);
        STAMP(2); K1_STEP(4); K1_STEP(5); K1_STEP(6); K1_STEP(7); STAMP(3);
#undef K1_STEP
    }
#pragma unroll
    for (int m = 0; m < 2; ++m)
#pragma unroll
        for (int nt = 0; nt < 2; ++nt) {
            int pq = nt * 16 + fq * 4;
            char* up = ubuf + (w * 2 + m) * USEG + fr * US + pq * 8;
            f32x4 re = acc[m][nt], im = acc[m][nt + 2];
            f32x4 v0 = {re[0], im[0], re[1], im[1]}, v1 = {re[2], im[2], re[3], im[3]};
            *reinterpret_cast<f32x4*>(up) = v0;
            *reinterpret_cast<f32x4*>(up + 16) = v1;
        }
    if (lane < 8) *reinterpret_cast<f32x4*>(pbuf + (w * 8 + lane) * 16) = pconst;
    LBAR();
    STAMP(4);
    {
        char* wimg = smem + 65536 + w * 8192;
        const int fbase = f0 & ~31;
        const int foff = (f0 & 31) ? 512 : 0;
#pragma unroll
        for (int n = 0; n < 7; ++n) {
            const int f = f0 + n * 16 + fq * 4;
            char* hp = wimg + ((f - fbase) >> 5) * 1024 + ((f >> 3) & 3) * 256 + fr * 16 + ((f >> 2) & 1) * 8 - foff;
#pragma unroll
            for (int m = 0; m < 2; ++m) {
                const f32x4 av = acc[m][4 + n];
                const f32x2 o0 = gelu_tanh2((f32x2){av[0], av[1]}), o1 = gelu_tanh2((f32x2){av[2], av[3]});
                u32x2 pk; pk.x = pack2(o0.x, o0.y); pk.y = pack2(o1.x, o1.y);
                *reinterpret_cast<u32x2*>(hp + m * 3584) = pk;
            }
        }
        STAMP(5);
        const int lw = l0 + w * 32;
        char* hrow = H_t + ((size_t)(lw >> 6) * KSF * 4 + ((lw >> 4) & 3) + (size_t)(fbase >> 5) * 4) * 1024;
#pragma unroll
        for (int i = 0; i < 7; ++i) {
            int m, o;
            if (i < 6) { m = i / 3; o = (i % 3) * 1024 + lane * 16; }
            else { m = lane >> 5; o = 3072 + (lane & 31) * 16; }
            u32x4 v = *reinterpret_cast<const u32x4*>(wimg + m * 3584 + o);
            const int og = o + foff;
            ST16(hrow + m * 1024 + (og >> 10) * 4096 + (og & 1023), v);
        }
    }
    STAMP_FLUSH(0);
}

constexpr int K2_RS = 272;
constexpr int K2_SLOT = 18432;
constexpr int K2_LDS = 8 * K2_SLOT;
constexpr int K2_NFW = 4;
constexpr int K2_NFS = 14;
constexpr int K2_NSS = 8;

__global__ __launch_bounds__(512, 2) void k2_gemm_mix(
    const bf16* __restrict__ x, const bf16* __restrict__ dskip, const bf16* __restrict__ b2,
    const char* __restrict__ hloc_t, const float* __restrict__ agg,
    const char* __restrict__ H_t, const char* __restrict__ w2_t, const char* __restrict__ c_t,
    const float* __restrict__ gate, const float* __restrict__ lamtab, float* __restrict__ out)
{
    extern __shared__ __attribute__((aligned(16))) char smem[];
    const int tid = threadIdx.x, lane = tid & 63;
    const int wv = __builtin_amdgcn_readfirstlane(tid >> 6);
    const int xcd = blockIdx.x & 7, ii = blockIdx.x >> 3;
    const int rt = xcd * 4 + (ii & 3);
    const int ct = ii >> 2;
    const int l0 = rt * 64, d0 = ct * 64;
    const int fr = lane & 15, fq = lane >> 4;
    STAMP_DECL();
    const bool is_ffn = wv < K2_NFW;
    const int k0 = is_ffn ? wv : wv - K2_NFW;
    const int kst = is_ffn ? K2_NFW : 8 - K2_NFW;
    const int nst = is_ffn ? (KSF - k0 + kst - 1) / kst : (KSS - k0 + kst - 1) / kst;
    const char* pA = (is_ffn ? H_t + (size_t)(rt * KSF + k0) * 4096 : hloc_t + (size_t)(rt * KSS + k0) * 4096) + lane * 16;
    const char* pB = (is_ffn ? w2_t + (size_t)(ct * KSF + k0) * 4096 : c_t + (size_t)(ct * KSS + k0) * 4096) + lane * 16;
    const int sstride = kst * 4096;

    f32x4 acc[4][4];
#pragma unroll
    for (int n = 0; n < 4; ++n)
#pragma unroll
        for (int m = 0; m < 4; ++m) acc[n][m] = (f32x4){0.f, 0.f, 0.f, 0.f};

    float gl[4];
#pragma unroll
    for (int m = 0; m < 4; ++m) gl[m] = gate[l0 + m * 16 + fr];

    if (is_ffn) {
        constexpr int DP = 4;
        bf16x8 fa[DP][4], fb[DP][4];
        auto load_step = [&](int s) {
            const int slot = s % DP;
#pragma unroll
            for (int i = 0; i < 4; ++i) fa[slot][i] = *reinterpret_cast<const bf16x8*>(pA + (size_t)s * sstride + i * 1024);
#pragma unroll
            for (int i = 0; i < 4; ++i) fb[slot][i] = *reinterpret_cast<const bf16x8*>(pB + (size_t)s * sstride + i * 1024);
        };
        PIN();
#pragma unroll
        for (int s = 0; s < DP - 1; ++s) load_step(s);
        PIN();
#pragma unroll
        for (int s = 0; s < K2_NFS; ++s) {
            if (s + DP - 1 < K2_NFS) load_step(s + DP - 1);
            PIN();
            const int slot = s % DP;
#pragma unroll
            for (int n = 0; n < 4; ++n)
#pragma unroll
                for (int m = 0; m < 4; ++m) acc[n][m] = __builtin_amdgcn_mfma_f32_16x16x32_bf16(fb[slot][n], fa[slot][m], acc[n][m], 0, 0, 0);
            PIN();
        }
        STAMP(3);
#pragma unroll
        for (int m = 0; m < 4; ++m) gl[m] = 1.f - gl[m];
    } else {
        __builtin_amdgcn_s_setprio(2);
        constexpr int DP = 3;
        bf16x8 fa[DP][4], fb[DP][4];
        auto load_step = [&](int s) {
            const int slot = s % DP;
#pragma unroll
            for (int i = 0; i < 4; ++i) fa[slot][i] = *reinterpret_cast<const bf16x8*>(pA + (size_t)s * sstride + i * 1024);
#pragma unroll
            for (int i = 0; i < 4; ++i) fb[slot][i] = *reinterpret_cast<const bf16x8*>(pB + (size_t)s * sstride + i * 1024);
        };
        char* T1w = smem + wv * K2_SLOT;
        char* L16w = T1w + 17408;
        {
            const int chunk = rt >> 1, jbase = (rt & 1) * 64 + 1;
            f32x4 lt0[2], lt1[2]; f32x2 ag[2][NCH - 1];
#pragma unroll
            for (int h = 0; h < 2; ++h) {
                const int q = lane + h * 64;
                const int p = ((q >> 4) * 4 + k0) * 16 + (q & 15);
                lt0[h] = *reinterpret_cast<const f32x4*>(lamtab + (size_t)p * 8);
                lt1[h] = *reinterpret_cast<const f32x4*>(lamtab + (size_t)p * 8 + 4);
#pragma unroll
                for (int c = 0; c < NCH - 1; ++c) ag[h][c] = *reinterpret_cast<const f32x2*>(agg + ((size_t)c * P_ + p) * 2);
            }
            PIN();
            load_step(0);
            PIN();
#pragma unroll
            for (int h = 0; h < 2; ++h) {
                const int q = lane + h * 64;
                const f32x2 lam = {lt0[h][0], lt0[h][1]}, l16 = {lt0[h][2], lt0[h][3]};
                const f32x2 l64 = {lt1[h][0], lt1[h][1]}, l128 = {lt1[h][2], lt1[h][3]};
                f32x2 S = {0.f, 0.f};
#pragma unroll
                for (int c = 0; c < NCH - 1; ++c) {
                    if (c < chunk) { f32x2 t = cmul(l128, S); S.x = t.x + ag[h][c].x; S.y = t.y + ag[h][c].y; }
                }
                f32x2 t = cmul(S, lam);
                if (jbase > 1) t = cmul(t, l64);
#pragma unroll
                for (int r = 0; r < 16; ++r) { *reinterpret_cast<f32x2*>(T1w + (r * 128 + q) * 8 + r * 16) = t; t = cmul(t, lam); }
                *reinterpret_cast<f32x2*>(L16w + q * 8) = l16;
            }
        }
        STAMP(1);
        PIN();
#pragma unroll
        for (int s = 1; s < DP - 1; ++s) load_step(s);
        PIN();
        asm volatile("s_waitcnt lgkmcnt(0)" ::: "memory");
        STAMP(2);
        const char* T1b = T1w + (fr * 128 + fq * 4) * 8 + fr * 16;
        const char* L16b = L16w + (fq * 4) * 8;
#pragma unroll
        for (int s = 0; s < K2_NSS; ++s) {
            if (s + DP - 1 < K2_NSS) load_step(s + DP - 1);
            PIN();
            const int slot = s % DP;
            const int pofs = s * 16 * 8;
            f32x4 t01 = *reinterpret_cast<const f32x4*>(T1b + pofs), t23 = *reinterpret_cast<const f32x4*>(T1b + pofs + 16);
            f32x4 m01 = *reinterpret_cast<const f32x4*>(L16b + pofs), m23 = *reinterpret_cast<const f32x4*>(L16b + pofs + 16);
            f32x2 t[4] = {{t01[0], t01[1]}, {t01[2], t01[3]}, {t23[0], t23[1]}, {t23[2], t23[3]}};
            const f32x2 mm[4] = {{m01[0], m01[1]}, {m01[2], m01[3]}, {m23[0], m23[1]}, {m23[2], m23[3]}};
#pragma unroll
            for (int m = 0; m < 4; ++m) {
                u32x4 o;
#pragma unroll
                for (int j = 0; j < 4; ++j) {
                    o[j] = pack2(t[j].x, t[j].y);
                    if (m < 3) t[j] = cmul(t[j], mm[j]);
                }
                const bf16x8 tf = __builtin_bit_cast(bf16x8, o);
#pragma unroll
                for (int n = 0; n < 4; ++n) {
                    acc[n][m] = __builtin_amdgcn_mfma_f32_16x16x32_bf16(fb[slot][n], fa[slot][m], acc[n][m], 0, 0, 0);
                    acc[n][m] = __builtin_amdgcn_mfma_f32_16x16x32_bf16(fb[slot][n], tf, acc[n][m], 0, 0, 0);
                }
            }
            PIN();
        }
        STAMP(3);
#pragma unroll
        for (int m = 0; m < 4; ++m) gl[m] = 2.f * gl[m];
    }
    {
        char* red = smem + wv * K2_SLOT;
#pragma unroll
        for (int m = 0; m < 4; ++m)
#pragma unroll
            for (int n = 0; n < 4; ++n) {
                f32x4 v;
#pragma unroll
                for (int j = 0; j < 4; ++j) v[j] = gl[m] * acc[n][m][j];
                *reinterpret_cast<f32x4*>(red + (m * 16 + fr) * K2_RS + (n * 16 + fq * 4) * 4) = v;
            }
    }
    const int c4 = (tid & 15) * 4;
    const uint2 dv = *reinterpret_cast<const uint2*>(dskip + d0 + c4);
    const uint2 bv = *reinterpret_cast<const uint2*>(b2 + d0 + c4);
    uint2 xv[2]; float gr[2];
#pragma unroll
    for (int i = 0; i < 2; ++i) {
        const int row = (tid >> 4) + 32 * i;
        xv[i] = *reinterpret_cast<const uint2*>(x + (size_t)(l0 + row) * D_ + d0 + c4);
        gr[i] = gate[l0 + row];
    }
    LBAR();
    STAMP(4);
    {
        const float df[4] = {bflo(dv.x), bfhi(dv.x), bflo(dv.y), bfhi(dv.y)};
        const float bf[4] = {bflo(bv.x), bfhi(bv.x), bflo(bv.y), bfhi(bv.y)};
#pragma unroll
        for (int i = 0; i < 2; ++i) {
            const int row = (tid >> 4) + 32 * i;
            const float g = gr[i];
            const float xf[4] = {bflo(xv[i].x), bfhi(xv[i].x), bflo(xv[i].y), bfhi(xv[i].y)};
            f32x4 sacc = *reinterpret_cast<const f32x4*>(smem + row * K2_RS + c4 * 4);
#pragma unroll
            for (int ww = 1; ww < 8; ++ww) sacc += *reinterpret_cast<const f32x4*>(smem + ww * K2_SLOT + row * K2_RS + c4 * 4);
            f32x4 o;
#pragma unroll
            for (int j = 0; j < 4; ++j) o[j] = sacc[j] + g * xf[j] * df[j] + (1.f - g) * bf[j];
            *reinterpret_cast<f32x4*>(out + (size_t)(l0 + row) * D_ + d0 + c4) = o;
        }
    }
    if (wv == 0 || wv == 5) { STAMP_FLUSH_W(wv ? 3 : 1); }
}

extern "C" void kernel_launch(void* const* d_in, const int* in_sizes, int n_in,
                              void* d_out, int out_size, void* d_ws, size_t ws_size,
                              hipStream_t stream) {
    const bf16* x = (const bf16*)d_in[0];
    const bf16* lre = (const bf16*)d_in[1];
    const bf16* lim = (const bf16*)d_in[2];
    const bf16* bre = (const bf16*)d_in[3];
    const bf16* bim = (const bf16*)d_in[4];
    const bf16* cre = (const bf16*)d_in[5];
    const bf16* cim = (const bf16*)d_in[6];
    const bf16* logdt = (const bf16*)d_in[7];
    const bf16* dskip = (const bf16*)d_in[8];
    const bf16* w1 = (const bf16*)d_in[10];
    const bf16* b1 = (const bf16*)d_in[11];
    const bf16* w2 = (const bf16*)d_in[12];
    const bf16* b2 = (const bf16*)d_in[13];
    const bf16* gw = (const bf16*)d_in[14];

    char* ws = (char*)d_ws;
    char* hloc_t = ws;
    char* H_t = ws + (4u << 20);
    char* w2_t = ws + (12u << 20);
    char* c_t = ws + (14u << 20);
    float* agg = (float*)(ws + (16u << 20));
    float* gate = (float*)(ws + (16u << 20) + 65536);
    float* lamtab = (float*)(ws + (16u << 20) + 65536 + 8192);

    (void)hipFuncSetAttribute((const void*)k1_gemm_scan, hipFuncAttributeMaxDynamicSharedMemorySize, K1_LDS);
    (void)hipFuncSetAttribute((const void*)k2_gemm_mix, hipFuncAttributeMaxDynamicSharedMemorySize, K2_LDS);
    k1_gemm_scan<<<256, 512, K1_LDS, stream>>>(x, lre, lim, bre, bim, logdt, w1, b1, w2, cre, cim, gw, hloc_t, agg, H_t, w2_t, c_t, gate, lamtab);
    k2_gemm_mix<<<256, 512, K2_LDS, stream>>>(x, dskip, b2, hloc_t, agg, H_t, w2_t, c_t, gate, lamtab, (float*)d_out);
}
```
